```python
import torch

def _softmax(x, axis=-1):
    return torch.softmax(x, dim=axis)

def _gelu(x):
    return torch.nn.functional.gelu(x, approximate='tanh')

def _silu(x):
    return torch.nn.functional.silu(x)

def _sigmoid(x):
    return torch.sigmoid(x)

def _layernorm(x, axis=-1, eps=1e-05):
    m = x.mean(dim=axis, keepdim=True)
    v = x.var(dim=axis, keepdim=True, unbiased=False)
    return (x - m) / torch.sqrt(v + eps)

def _scatter_add(tgt, idx, src, axis=0):
    return tgt.clone().index_add_(axis, idx, src)

def _segment_sum(src, seg, n):
    out = torch.zeros((n,) + src.shape[1:], dtype=src.dtype)
    return out.index_add_(0, seg, src)

def _pad(x, pw):
    flat = tuple((v for pair in reversed(pw) for v in pair))
    return torch.nn.functional.pad(x, flat)
kernel_name = 'adaLN_cond__memory_aug_recurrent_mem_tr__mixer_gmlp__prompt'

def setup_inputs():

    _w = lambda a: (lambda t: t.to(torch.bfloat16) if t.is_floating_point() else t)(torch.as_tensor(a))

    def _A_setup():
        B, S, Dm = (1, 2048, 576)
        H, Dh = (32, 128)
        M, seg = (8, 32)
        seg = min(seg, S)
        S = S // seg * seg
        return {'x': _w((lambda *s: torch.randn(*s, dtype=torch.bfloat16) * 0.05)(B, S, Dm)), 'mem0': _w((lambda *s: torch.randn(*s, dtype=torch.bfloat16) * 0.4)(B, M, Dm)), 'Wq': _w((lambda *s: torch.randn(*s, dtype=torch.bfloat16) * 3.2)(Dm, H, Dh) * Dm ** (-0.5)), 'Wk': _w((lambda *s: torch.randn(*s, dtype=torch.bfloat16) * 3.2)(Dm, H, Dh) * Dm ** (-0.5)), 'Wv': _w((lambda *s: torch.randn(*s, dtype=torch.bfloat16) * 0.4)(Dm, H, Dh) * Dm ** (-0.5)), 'Wo': _w((lambda *s: torch.randn(*s, dtype=torch.bfloat16) * 0.4)(H, Dh, Dm) * (H * Dh) ** (-0.5)), 'seg_len': seg}
    _A_in = _A_setup()

    def _B_setup():
        B, S, D, F = (1, 2048, 576, 4608)
        Fh = F // 2
        return {'x': _w((lambda *s: torch.randn(*s, dtype=torch.bfloat16) * 0.05)(B, S, D)), 'w_in': _w((lambda *s: torch.randn(*s, dtype=torch.bfloat16) * 0.8)(D, 2 * Fh)), 'w_sp': _w(0.01 * (lambda *s: torch.randn(*s, dtype=torch.bfloat16) * 0.8)(S, S)), 'b_sp': _w(1.0 + 0.01 * (lambda *s: torch.randn(*s, dtype=torch.bfloat16) * 0.05)(S)), 'w_out': _w((lambda *s: torch.randn(*s, dtype=torch.bfloat16) * 0.05)(Fh, D))}
    _B_in = _B_setup()
    return {'A_x': _A_in['x'], 'A_mem0': _A_in['mem0'], 'A_Wq': _A_in['Wq'], 'A_Wk': _A_in['Wk'], 'A_Wv': _A_in['Wv'], 'A_Wo': _A_in['Wo'], 'A_seg_len': _A_in['seg_len'], 'B_x': _B_in['x'], 'B_w_in': _B_in['w_in'], 'B_w_sp': _B_in['w_sp'], 'B_b_sp': _B_in['b_sp'], 'B_w_out': _B_in['w_out'], 'c': _w((lambda *s: torch.randn(*s, dtype=torch.bfloat16) * 0.1)(1, 576) * 8.0), 'w_mod': _w((lambda *s: torch.randn(*s, dtype=torch.bfloat16) * 0.1)(576, 6 * 576) * 576 ** (-0.5) * 8.0)}

def reference(A_x, A_mem0, A_Wq, A_Wk, A_Wv, A_Wo, A_seg_len, B_x, B_w_in, B_w_sp, B_b_sp, B_w_out, c, w_mod):

    def _A_ref(x, mem0, Wq, Wk, Wv, Wo, seg_len):
        B, S, Dm = x.shape
        H, Dh = (Wq.shape[1], Wq.shape[2])
        M = mem0.shape[1]
        scale = Dh ** (-0.5)
        n_seg = S // seg_len
        L = M + seg_len
        idx = torch.arange(L)
        mask = (idx[:, None] < M) | (idx[:, None] >= idx[None, :])
        mem = mem0
        outs = []
        for s in range(n_seg):
            xs = x[:, s * seg_len:(s + 1) * seg_len, :].clone()
            h = torch.concatenate([mem, xs], axis=1)
            q = torch.einsum('btd,dhe->bhte', h, Wq)
            k = torch.einsum('btd,dhe->bhte', h, Wk)
            v = torch.einsum('btd,dhe->bhte', h, Wv)
            scores = torch.einsum('bhqe,bhke->bhqk', q, k) * scale
            scores = torch.where(mask[None, None], scores, -1000000000.0)
            p = _softmax(scores, axis=-1)
            o = torch.einsum('bhqk,bhke->bhqe', p, v)
            o = torch.einsum('bhte,hed->btd', o, Wo)
            mem = o[:, :M, :].clone()
            outs.append(o[:, M:, :])
        return torch.concatenate(outs, axis=1)

    def _B_ref(x, w_in, w_sp, b_sp, w_out):
        B, S, D = x.shape
        F = w_in.shape[-1]
        Fh = F // 2
        z = _gelu(torch.einsum('bsd,df->bsf', x, w_in))
        z1 = z[..., :Fh].clone()
        z2 = _layernorm(z[..., Fh:], axis=-1)
        g = torch.einsum('ts,bsf->btf', w_sp, z2) + b_sp[None, :, None]
        return torch.einsum('bsf,fd->bsd', z1 * g, w_out)
    x = A_x
    mod = (_silu(c) @ w_mod).reshape(c.shape[0], 6, 1, -1)
    sa, ca, ga, sb, cb, gb = (mod[:, 0].clone(), mod[:, 1].clone(), mod[:, 2].clone(), mod[:, 3].clone(), mod[:, 4].clone(), mod[:, 5].clone())
    h = _layernorm(x, axis=-1) * (1 + ca) + sa
    x = x + ga * _A_ref(h, A_mem0, A_Wq, A_Wk, A_Wv, A_Wo, A_seg_len)
    h = _layernorm(x, axis=-1) * (1 + cb) + sb
    x = x + gb * _B_ref(h, B_w_in, B_w_sp, B_b_sp, B_w_out)
    return x
```

```cpp
#include <hip/hip_runtime.h>
#include <stdint.h>

#define GAS __attribute__((address_space(1)))
#define LAS __attribute__((address_space(3)))
typedef unsigned short u16;
typedef __bf16 bf16x2 __attribute__((ext_vector_type(2)));
typedef __bf16 bf16x8 __attribute__((ext_vector_type(8)));
typedef __bf16 bf16x16 __attribute__((ext_vector_type(16)));
typedef short s16x4 __attribute__((ext_vector_type(4)));
typedef float f32x2 __attribute__((ext_vector_type(2)));
typedef float f32x4 __attribute__((ext_vector_type(4)));
typedef float f32x16 __attribute__((ext_vector_type(16)));
typedef unsigned u32x2 __attribute__((ext_vector_type(2)));
typedef unsigned u32x4 __attribute__((ext_vector_type(4)));
typedef unsigned u32x8 __attribute__((ext_vector_type(8)));
typedef LAS unsigned char* lptr;
typedef __amdgpu_buffer_rsrc_t rsrc_t;

constexpr int S_ = 2048, D_ = 576, HD_ = 4096, NSEG = 64, F_ = 4608, FH = 2304;
constexpr unsigned WS_PROG = 0;
constexpr unsigned WS_TICK = 32768;
constexpr unsigned WS_OVF = WS_TICK + 16 * 128;
constexpr unsigned WS_PAR = WS_TICK + 17 * 128;
constexpr unsigned WS_ABORT = WS_TICK + 18 * 128;
constexpr unsigned WS_CBAR = WS_TICK + 19 * 128;
constexpr unsigned WS_LNP = 36864;
constexpr unsigned WS_CTRL_BYTES = 40960;
constexpr unsigned WS_MOD = 65536;
constexpr unsigned WS_H = 131072;
constexpr unsigned WS_X1 = WS_H + S_ * D_ * 2;
constexpr unsigned WS_H2 = WS_X1 + S_ * D_ * 2;
constexpr unsigned WS_QKV = WS_H2 + S_ * D_ * 2;
constexpr unsigned WS_OX = WS_QKV + S_ * 12288 * 2;
constexpr unsigned WS_PT = WS_OX + S_ * HD_ * 2;
constexpr unsigned WS_Z = WS_PT + NSEG * 4 * 32 * D_ * 4;
constexpr unsigned WS_ZS = WS_Z + S_ * F_ * 2;
constexpr unsigned WS_Y = WS_ZS + NSEG * 18 * 32 * 8;
constexpr unsigned WS_EX = WS_Y + S_ * FH * 2;
constexpr unsigned WS_END = WS_EX + 3 * 2097152;
constexpr unsigned WS_DBG = WS_END;
constexpr int R_CHAIN = 0, R_T4 = 64, R_T5 = 100, R_LN = 136, R_GQ = 144, N_GQ = 112, N_G = 96;
constexpr unsigned EMPTY = 0xFFFFFFFFu;
constexpr unsigned SPIN_CAP = 1u << 17;

struct Params {
  const u16* x; const u16* mem0; const u16* wq; const u16* wk; const u16* wv; const u16* wo;
  const u16* win; const u16* wsp; const u16* bsp; const u16* wout;
  unsigned char* ws; u16* out;
  unsigned long long wsbytes;
  unsigned rolemask; unsigned tune0; unsigned tune1; unsigned tune2;
};

#define MEMBAR() asm volatile("" ::: "memory")
#define WAIT_VM0() asm volatile("s_waitcnt vmcnt(0)" ::: "memory")
#define RAW_BAR() do { asm volatile("" ::: "memory"); __builtin_amdgcn_s_barrier(); asm volatile("" ::: "memory"); } while (0)
#define WG_BAR() do { asm volatile("s_waitcnt lgkmcnt(0)" ::: "memory"); __builtin_amdgcn_s_barrier(); asm volatile("" ::: "memory"); } while (0)
__device__ __forceinline__ unsigned pk2(float a, float b) { bf16x2 v = {(__bf16)a, (__bf16)b}; return __builtin_bit_cast(unsigned, v); }
__device__ __forceinline__ float rbf(float a) { return (float)(__bf16)a; }
__device__ __forceinline__ float blo(unsigned u) { return __builtin_bit_cast(float, u << 16); }
__device__ __forceinline__ float bhi(unsigned u) { return __builtin_bit_cast(float, u & 0xffff0000u); }
__device__ __forceinline__ float bf2f(u16 u) { return __builtin_bit_cast(float, (unsigned)u << 16); }
__device__ __forceinline__ rsrc_t mk_rsrc(const void* p, unsigned long long bytes) {
  const unsigned long long a = (unsigned long long)p;
  const unsigned lo = __builtin_amdgcn_readfirstlane((unsigned)a), hi = __builtin_amdgcn_readfirstlane((unsigned)(a >> 32));
  const unsigned n = __builtin_amdgcn_readfirstlane((unsigned)(bytes > 0xFFFFFFF0ull ? 0xFFFFFFF0ull : bytes));
  return __builtin_amdgcn_make_buffer_rsrc((void*)(((unsigned long long)hi << 32) | lo), 0, n, 0x00020000);
}
constexpr int SC1 = 16;
__device__ __forceinline__ u32x4 ld16c(rsrc_t r, unsigned vo, unsigned so = 0) { return __builtin_amdgcn_raw_buffer_load_b128(r, vo, so, SC1); }
__device__ __forceinline__ u32x4 ld16p(rsrc_t r, unsigned vo, unsigned so = 0) { return __builtin_amdgcn_raw_buffer_load_b128(r, vo, so, 0); }
__device__ __forceinline__ u32x2 ld8c(rsrc_t r, unsigned vo, unsigned so = 0) { return __builtin_amdgcn_raw_buffer_load_b64(r, vo, so, SC1); }
__device__ __forceinline__ unsigned ld4c(rsrc_t r, unsigned vo, unsigned so = 0) { return __builtin_amdgcn_raw_buffer_load_b32(r, vo, so, SC1); }
__device__ __forceinline__ void st16c(rsrc_t r, unsigned vo, unsigned so, u32x4 v) { __builtin_amdgcn_raw_buffer_store_b128(v, r, vo, so, SC1); }
__device__ __forceinline__ void st16p(rsrc_t r, unsigned vo, unsigned so, u32x4 v) { __builtin_amdgcn_raw_buffer_store_b128(v, r, vo, so, 0); }
__device__ __forceinline__ void st8c(rsrc_t r, unsigned vo, unsigned so, u32x2 v) { __builtin_amdgcn_raw_buffer_store_b64(v, r, vo, so, SC1); }
__device__ __forceinline__ void st4c(rsrc_t r, unsigned vo, unsigned so, unsigned v) { __builtin_amdgcn_raw_buffer_store_b32(v, r, vo, so, SC1); }
template <class T> __device__ __forceinline__ T lds_ld(lptr p) { return *(LAS T*)p; }
template <class T> __device__ __forceinline__ void lds_st(lptr p, T v) { *(LAS T*)p = v; }
typedef short v4i16_t __attribute__((ext_vector_type(4)));
__device__ __forceinline__ u32x2 lds_tr(lptr p) { return __builtin_bit_cast(u32x2, __builtin_amdgcn_ds_read_tr16_b64_v4i16((LAS v4i16_t*)p)); }
__device__ __forceinline__ void glds16(const void* gsrc, unsigned lds_dst) {
  unsigned keep;
  asm volatile("s_mov_b32 %0, m0\n\ts_mov_b32 m0, %2\n\ts_nop 0\n\tglobal_load_lds_dwordx4 %1, off\n\ts_mov_b32 m0, %0" : "=&s"(keep) : "v"(gsrc), "s"(lds_dst) : "memory");
}
__device__ __forceinline__ void glds16c(const void* sbase, unsigned voff, unsigned lds_dst) {
  unsigned keep;
  asm volatile("s_mov_b32 %0, m0\n\ts_mov_b32 m0, %3\n\ts_nop 0\n\tglobal_load_lds_dwordx4 %1, %2 sc1\n\ts_mov_b32 m0, %0" : "=&s"(keep) : "v"(voff), "s"(sbase), "s"(lds_dst) : "memory");
}
__device__ __forceinline__ f32x4 mfma16(u32x4 a, u32x4 b, f32x4 c) { return __builtin_amdgcn_mfma_f32_16x16x32_bf16(__builtin_bit_cast(bf16x8, a), __builtin_bit_cast(bf16x8, b), c, 0, 0, 0); }
__device__ __forceinline__ f32x4 mfma16k16(u32x2 a, u32x2 b, f32x4 c) { return __builtin_amdgcn_mfma_f32_16x16x16bf16_1k(__builtin_bit_cast(s16x4, a), __builtin_bit_cast(s16x4, b), c, 0, 0, 0); }
__device__ __forceinline__ f32x16 mfma32(u32x4 a, u32x4 b, f32x16 c) { return __builtin_amdgcn_mfma_f32_32x32x16_bf16(__builtin_bit_cast(bf16x8, a), __builtin_bit_cast(bf16x8, b), c, 0, 0, 0); }
__device__ __forceinline__ f32x4 smfmac(u32x4 a, u32x8 b, f32x4 c, int idx) { return __builtin_amdgcn_smfmac_f32_16x16x64_bf16(__builtin_bit_cast(bf16x8, a), __builtin_bit_cast(bf16x16, b), c, idx, 0, 0); }
__device__ __forceinline__ float xr16(float a, bool mx) {
  const unsigned u = __builtin_bit_cast(unsigned, a);
  const auto r = __builtin_amdgcn_permlane16_swap(u, u, false, false);
  const float p = __builtin_bit_cast(float, (unsigned)r[0]), q = __builtin_bit_cast(float, (unsigned)r[1]);
  return mx ? fmaxf(p, q) : p + q;
}
__device__ __forceinline__ float xr32(float a, bool mx) {
  const unsigned u = __builtin_bit_cast(unsigned, a);
  const auto r = __builtin_amdgcn_permlane32_swap(u, u, false, false);
  const float p = __builtin_bit_cast(float, (unsigned)r[0]), q = __builtin_bit_cast(float, (unsigned)r[1]);
  return mx ? fmaxf(p, q) : p + q;
}
template <int CTRL> __device__ __forceinline__ float dppf(float a) { return __builtin_bit_cast(float, __builtin_amdgcn_update_dpp(0, __builtin_bit_cast(int, a), CTRL, 0xf, 0xf, true)); }
__device__ __forceinline__ float wave_sum(float v) {
  v += dppf<0xB1>(v);
  v += dppf<0x4E>(v);
  v += dppf<0x141>(v);
  v += dppf<0x140>(v);
  v = xr16(v, false); v = xr32(v, false);
  return v;
}
#define LST(i) do {} while (0)
#define STAMP_CLK(i) do {} while (0)
__device__ __forceinline__ unsigned long long clk() { return __builtin_amdgcn_s_memtime(); }
#define ACC(k, a, b) do {} while (0)
#define ACLK(x) do {} while (0)
#define STAMP_AT(role, i) do {} while (0)
#define STAMP_CLK_OFF(i) do {} while (0)

__device__ __forceinline__ void wait_ge(rsrc_t rW, unsigned off, bool act, unsigned target) {
  for (unsigned sp = 0;; ++sp) {
    MEMBAR();
    const unsigned v = ld4c(rW, act ? off : WS_ABORT);
    const bool ok = act ? (v >= target) : true;
    if (__all(ok)) return;
    if ((sp & 63u) == 63u) {
      if (__builtin_amdgcn_readfirstlane(ld4c(rW, WS_ABORT)) != 0u) return;
      if (sp > SPIN_CAP) { st4c(rW, WS_ABORT, 0, 1u); return; }
    }
    __builtin_amdgcn_s_sleep(1);
  }
}

__global__ void __launch_bounds__(256) k_mod(const u16* __restrict__ c, const u16* __restrict__ wmod, float* __restrict__ mod) {
  __shared__ float sc[576];
  __shared__ float red[4][32];
  const int tid = threadIdx.x, b = blockIdx.x, lane = tid & 63, wave = tid >> 6;
  for (int i = tid; i < 576; i += 256) { const float v = bf2f(c[i]); sc[i] = rbf(v / (1.0f + __expf(-v))); }
  __syncthreads();
  const int kg = tid >> 2, cg = tid & 3;
  float acc[8];
#pragma unroll
  for (int j = 0; j < 8; ++j) acc[j] = 0.f;
  u32x4 w[9];
#pragma unroll
  for (int i = 0; i < 9; ++i) w[i] = *(const GAS u32x4*)(wmod + (size_t)(kg + 64 * i) * 3456 + 32 * b + 8 * cg);
#pragma unroll
  for (int i = 0; i < 9; ++i) {
    const float s = sc[kg + 64 * i];
    acc[0] += s * blo(w[i].x); acc[1] += s * bhi(w[i].x); acc[2] += s * blo(w[i].y); acc[3] += s * bhi(w[i].y);
    acc[4] += s * blo(w[i].z); acc[5] += s * bhi(w[i].z); acc[6] += s * blo(w[i].w); acc[7] += s * bhi(w[i].w);
  }
#pragma unroll
  for (int j = 0; j < 8; ++j) {
    float v = acc[j];
    v += __shfl_xor(v, 4); v += __shfl_xor(v, 8); v += __shfl_xor(v, 16); v += __shfl_xor(v, 32);
    if (lane < 4) red[wave][8 * cg + j] = v;
  }
  __syncthreads();
  if (tid < 32) mod[32 * b + tid] = rbf(red[0][tid] + red[1][tid] + red[2][tid] + red[3][tid]);
}

__device__ __forceinline__ void adaln_row(float (&v)[16], int lane, const float* __restrict__ cc, const float* __restrict__ ss, u32x4& o0, u32x4& o1) {
  const bool two = lane < 8;
  float s = 0.f;
#pragma unroll
  for (int j = 0; j < 8; ++j) s += v[j] + (two ? v[8 + j] : 0.f);
  const float mean = wave_sum(s) * (1.0f / 576.0f);
  float q = 0.f;
#pragma unroll
  for (int j = 0; j < 8; ++j) { const float d = v[j] - mean; q += d * d; const float e = v[8 + j] - mean; q += two ? e * e : 0.f; }
  const float var = wave_sum(q) * (1.0f / 576.0f);
  const float mb = rbf(mean), sd = rbf(__builtin_sqrtf(rbf(rbf(var) + 1e-5f)));
  float r[16];
#pragma unroll
  for (int j = 0; j < 16; ++j) {
    const int col = (j < 8 ? 8 * lane : 512 + 8 * (lane & 7)) + (j & 7);
    const float n = rbf(rbf(v[j] - mb) / sd);
    r[j] = rbf(n * rbf(1.0f + cc[col])) + ss[col];
  }
  o0 = u32x4{pk2(r[0], r[1]), pk2(r[2], r[3]), pk2(r[4], r[5]), pk2(r[6], r[7])};
  o1 = u32x4{pk2(r[8], r[9]), pk2(r[10], r[11]), pk2(r[12], r[13]), pk2(r[14], r[15])};
}
__device__ __forceinline__ void unpack8(u32x4 w, float* v) {
  v[0] = blo(w.x); v[1] = bhi(w.x); v[2] = blo(w.y); v[3] = bhi(w.y); v[4] = blo(w.z); v[5] = bhi(w.z); v[6] = blo(w.w); v[7] = bhi(w.w);
}
__global__ void __launch_bounds__(512) k_ln1(const u16* __restrict__ x, const float* __restrict__ mod, u16* __restrict__ h) {
  const int lane = threadIdx.x & 63, row = blockIdx.x * 8 + (threadIdx.x >> 6);
  float v[16];
  const u32x4 a = *(const GAS u32x4*)(x + (size_t)row * 576 + 8 * lane);
  const u32x4 b = *(const GAS u32x4*)(x + (size_t)row * 576 + 512 + 8 * (lane & 7));
  unpack8(a, v); unpack8(b, v + 8);
  u32x4 o0, o1;
  adaln_row(v, lane, mod + 576, mod, o0, o1);
  *(GAS u32x4*)(h + (size_t)row * 576 + 8 * lane) = o0;
  if (lane < 8) *(GAS u32x4*)(h + (size_t)row * 576 + 512 + 8 * lane) = o1;
}

__device__ __forceinline__ unsigned swz_tok(unsigned row, unsigned ch) { return row * 128u + ((ch ^ ((row >> 1) & 7u)) << 4); }
__device__ __forceinline__ unsigned swz_w(unsigned row, unsigned ch) { return row * 256u + ((ch ^ (((row & 3u) << 2) | ((row >> 2) & 3u))) << 4); }
__device__ __forceinline__ void qkv_tile(const Params& p, rsrc_t rW, lptr lds, int mi, int nj) {
  const int tid = threadIdx.x, lane = tid & 63, wave = tid >> 6;
  const int wn = wave & 1, wt = wave >> 1;
  const u16* wsel = nj < 32 ? p.wq : (nj < 64 ? p.wk : p.wv);
  const GAS u16* wsrc = (const GAS u16*)wsel + (nj & 31) * 128;
  const GAS u16* hsrc = (const GAS u16*)(p.ws + WS_H) + (size_t)mi * 128 * 576;
  unsigned gt[2], lt[2], gw[2], lw[2];
#pragma unroll
  for (int j = 0; j < 2; ++j) {
    const unsigned id = tid + 512 * j;
    gt[j] = (id >> 3) * 576 + (id & 7) * 8; lt[j] = swz_tok(id >> 3, id & 7);
    gw[j] = (id >> 4) * 4096 + (id & 15) * 8; lw[j] = 32768 + swz_w(id >> 4, id & 15);
  }
  u32x4 rt[2], rw[2];
#define QKV_LOAD(ks) do { _Pragma("unroll") for (int j = 0; j < 2; ++j) { rt[j] = *(const GAS u32x4*)(hsrc + gt[j] + (ks) * 64); rw[j] = *(const GAS u32x4*)(wsrc + gw[j] + (size_t)(ks) * 64 * 4096); } } while (0)
#define QKV_WRITE(buf) do { _Pragma("unroll") for (int j = 0; j < 2; ++j) { lds_st(lds + (buf) * 16384 + lt[j], rt[j]); lds_st(lds + (buf) * 16384 + lw[j], rw[j]); } } while (0)
  f32x16 acc[2];
#pragma unroll
  for (int i = 0; i < 2; ++i)
#pragma unroll
    for (int r = 0; r < 16; ++r) acc[i][r] = 0.f;
  const unsigned hh = lane >> 5, blk = (lane >> 4) & 1, q4 = (lane & 15) >> 2, p4 = lane & 3;
  QKV_LOAD(0);
  WG_BAR();
  QKV_WRITE(0);
  QKV_LOAD(1);
  WG_BAR();
  for (int ks = 0; ks < 9; ++ks) {
    const int buf = ks & 1;
    if (ks + 1 < 9) { QKV_WRITE(buf ^ 1); if (ks + 2 < 9) QKV_LOAD(ks + 2); }
    lptr tb = lds + buf * 16384, wb = lds + 32768 + buf * 16384;
#pragma unroll
    for (int k4 = 0; k4 < 4; ++k4) {
      const u32x4 bt = lds_ld<u32x4>(tb + swz_tok(wt * 32 + (lane & 31), 2 * k4 + hh));
#pragma unroll
      for (int i = 0; i < 2; ++i) {
        const unsigned c0 = (wn * 64 + i * 32) / 8 + 2 * blk + (p4 >> 1);
        const u32x2 a0 = lds_tr(wb + swz_w(16 * k4 + 8 * hh + q4, c0) + 8 * (p4 & 1));
        const u32x2 a1 = lds_tr(wb + swz_w(16 * k4 + 8 * hh + 4 + q4, c0) + 8 * (p4 & 1));
        acc[i] = mfma32(u32x4{a0.x, a0.y, a1.x, a1.y}, bt, acc[i]);
      }
    }
    WG_BAR();
  }
  lptr eb = lds + 65536;
#pragma unroll
  for (int i = 0; i < 2; ++i)
#pragma unroll
    for (int g = 0; g < 4; ++g) {
      const unsigned row = wt * 32 + (lane & 31), n = wn * 64 + i * 32 + 8 * g + 4 * hh;
      lds_st(eb + row * 256 + (((n >> 3) ^ (row & 15)) << 4) + (n & 4) * 2, u32x2{pk2(acc[i][4 * g], acc[i][4 * g + 1]), pk2(acc[i][4 * g + 2], acc[i][4 * g + 3])});
    }
  WG_BAR();
#pragma unroll
  for (int j = 0; j < 4; ++j) {
    const unsigned id = tid + 512 * j, row = id >> 4, ch = id & 15;
    const u32x4 v = lds_ld<u32x4>(eb + row * 256 + ((ch ^ (row & 15)) << 4));
    st16c(rW, WS_QKV + (mi * 128 + row) * 24576u + nj * 256u + ch * 16, 0, v);
  }
#undef QKV_LOAD
#undef QKV_WRITE
}

constexpr unsigned L_WO = 0, L_AIM = 73728, L_QIM = 82944, L_KIM = 85120, L_VIM = 87296, L_VPX = 88320, L_OIM = 90368, L_TILE = 91520,
                   TILE_B = 20480, T_K = 0, T_Q = 8192, T_V = 16384, L_OXS = 136576, L_CNT = 141184, L_STAMP = 141312, L_WV = 141824, L_STG = 73728;
__device__ __forceinline__ int opaque(int v) { asm volatile("" : "+v"(v)); return v; }
#define SCHED() __builtin_amdgcn_sched_barrier(0)
#define UNI(x) ((unsigned)__builtin_amdgcn_readfirstlane((int)(x)))
constexpr unsigned P_UNIT = 16384;
constexpr float SCALE = 0.08838834764831845f, LOG2E = 1.4426950408889634f;
__device__ __forceinline__ unsigned umax4(u32x4 v) { return max(max(v.x, v.y), max(v.z, v.w)); }
__device__ __forceinline__ float fmax2(float a, float b) { return __builtin_elementwise_maximum(a, b); }
__device__ __forceinline__ float xmax16(float a) { const unsigned u = __builtin_bit_cast(unsigned, a); const auto r = __builtin_amdgcn_permlane16_swap(u, u, false, false);
  return fmax2(__builtin_bit_cast(float, (unsigned)r[0]), __builtin_bit_cast(float, (unsigned)r[1])); }
__device__ __forceinline__ float xmax32(float a) { const unsigned u = __builtin_bit_cast(unsigned, a); const auto r = __builtin_amdgcn_permlane32_swap(u, u, false, false);
  return fmax2(__builtin_bit_cast(float, (unsigned)r[0]), __builtin_bit_cast(float, (unsigned)r[1])); }

__device__ __forceinline__ void poll8(u32x4 (&v)[8], unsigned bad, unsigned vo, u32x4 rs, unsigned sA, unsigned sB) {
  asm volatile(
      "s_bitcmp1_b32 %[bad], 0\n\ts_cbranch_scc0 Lp%=_0\n\tbuffer_load_dwordx4 %0, %[vo], %[rs], %[sA] offen sc1\nLp%=_0:\n\t"
      "s_bitcmp1_b32 %[bad], 1\n\ts_cbranch_scc0 Lp%=_1\n\tbuffer_load_dwordx4 %1, %[vo], %[rs], %[sA] offen offset:1024 sc1\nLp%=_1:\n\t"
      "s_bitcmp1_b32 %[bad], 2\n\ts_cbranch_scc0 Lp%=_2\n\tbuffer_load_dwordx4 %2, %[vo], %[rs], %[sA] offen offset:2048 sc1\nLp%=_2:\n\t"
      "s_bitcmp1_b32 %[bad], 3\n\ts_cbranch_scc0 Lp%=_3\n\tbuffer_load_dwordx4 %3, %[vo], %[rs], %[sA] offen offset:3072 sc1\nLp%=_3:\n\t"
      "s_bitcmp1_b32 %[bad], 4\n\ts_cbranch_scc0 Lp%=_4\n\tbuffer_load_dwordx4 %4, %[vo], %[rs], %[sB] offen sc1\nLp%=_4:\n\t"
      "s_bitcmp1_b32 %[bad], 5\n\ts_cbranch_scc0 Lp%=_5\n\tbuffer_load_dwordx4 %5, %[vo], %[rs], %[sB] offen offset:1024 sc1\nLp%=_5:\n\t"
      "s_bitcmp1_b32 %[bad], 6\n\ts_cbranch_scc0 Lp%=_6\n\tbuffer_load_dwordx4 %6, %[vo], %[rs], %[sB] offen offset:2048 sc1\nLp%=_6:\n\t"
      "s_bitcmp1_b32 %[bad], 7\n\ts_cbranch_scc0 Lp%=_7\n\tbuffer_load_dwordx4 %7, %[vo], %[rs], %[sB] offen offset:3072 sc1\nLp%=_7:\n\t"
      "s_waitcnt vmcnt(0)"
      : "+v"(v[0]), "+v"(v[1]), "+v"(v[2]), "+v"(v[3]), "+v"(v[4]), "+v"(v[5]), "+v"(v[6]), "+v"(v[7])
      : [bad] "s"(bad), [vo] "v"(vo), [rs] "s"(rs), [sA] "s"(sA), [sB] "s"(sB)
      : "memory", "scc");
}
__device__ __forceinline__ void poll5(u32x4 (&v)[5], unsigned bad, unsigned vo, u32x4 rs, unsigned s0, unsigned s2, unsigned s4) {
  asm volatile(
      "s_bitcmp1_b32 %[bad], 0\n\ts_cbranch_scc0 Lq%=_0\n\tbuffer_load_dwordx4 %0, %[vo], %[rs], %[s0] offen sc1\nLq%=_0:\n\t"
      "s_bitcmp1_b32 %[bad], 1\n\ts_cbranch_scc0 Lq%=_1\n\tbuffer_load_dwordx4 %1, %[vo], %[rs], %[s0] offen offset:2048 sc1\nLq%=_1:\n\t"
      "s_bitcmp1_b32 %[bad], 2\n\ts_cbranch_scc0 Lq%=_2\n\tbuffer_load_dwordx4 %2, %[vo], %[rs], %[s2] offen sc1\nLq%=_2:\n\t"
      "s_bitcmp1_b32 %[bad], 3\n\ts_cbranch_scc0 Lq%=_3\n\tbuffer_load_dwordx4 %3, %[vo], %[rs], %[s2] offen offset:2048 sc1\nLq%=_3:\n\t"
      "s_bitcmp1_b32 %[bad], 4\n\ts_cbranch_scc0 Lq%=_4\n\tbuffer_load_dwordx4 %4, %[vo], %[rs], %[s4] offen sc1\nLq%=_4:\n\t"
      "s_waitcnt vmcnt(0)"
      : "+v"(v[0]), "+v"(v[1]), "+v"(v[2]), "+v"(v[3]), "+v"(v[4])
      : [bad] "s"(bad), [vo] "v"(vo), [rs] "s"(rs), [s0] "s"(s0), [s2] "s"(s2), [s4] "s"(s4)
      : "memory", "scc");
}
__device__ __forceinline__ void chain_stage_w(const Params& p, int h, int z, int kb, unsigned dst, int wave, int lane) {
#pragma unroll
  for (int j = 0; j < 5; ++j) {
    const int id = wave * 5 + j;
    if (id < 32) {
      const int row = (id & 15) * 4 + (lane >> 4), ch = (lane & 15) ^ (((row & 1) | (((row >> 3) & 3) << 1)) << 1);
      const u16* src = (id < 16 ? p.wq : p.wk) + (size_t)(kb * 64 + row) * 4096 + h * 128 + ch * 8;
      glds16(src, dst + id * 1024);
    } else {
      const int row = (id - 32) * 8 + (lane >> 3), ch = (lane & 7) ^ (((row >> 3) & 3) << 1);
      const u16* src = p.wv + (size_t)(kb * 64 + row) * 4096 + h * 128 + z * 64 + ch * 8;
      glds16(src, dst + id * 1024);
    }
  }
}
template <int PITCH> __device__ __forceinline__ u32x8 chain_read_w(lptr img, int cw, int lane) {
  const int gb = lane >> 4, q = (lane & 15) >> 2, pp = lane & 3;
  u32x2 t[4];
#pragma unroll
  for (int a = 0; a < 4; ++a) {
    const int row = 32 * (a & 1) + 16 * (q & 1) + 8 * (q >> 1) + gb + 4 * (a >> 1);
    const int sw = PITCH == 256 ? (((row & 1) | (((row >> 3) & 3) << 1)) << 1) : (((row >> 3) & 3) << 1);
    t[a] = lds_tr(img + row * PITCH + (((2 * cw + (pp >> 1)) ^ sw) << 4) + 8 * (pp & 1));
  }
  return u32x8{t[0].x, t[0].y, t[1].x, t[1].y, t[2].x, t[2].y, t[3].x, t[3].y};
}
__device__ __forceinline__ u32x8 chain_wo_frag(lptr f) {
  const u32x4 a = lds_ld<u32x4>(f), b = lds_ld<u32x4>(f + 1024);
  return u32x8{a.x, a.y, a.z, a.w, b.x, b.y, b.z, b.w};
}
__device__ __forceinline__ unsigned chain_pk_tile(f32x4 d) { return min(pk2(d[0] + d[2], d[1] + d[3]), 0xFFFEFFFFu); }

__device__ __forceinline__ void chain_wait_tiles(rsrc_t rW, int h, int seg, int lane_) {
  const int lane = opaque(lane_);
  const int T = (seg >> 2) * 96 + (lane < 3 ? lane : 0) * 32 + h;
  wait_ge(rW, WS_PROG + (R_GQ + T % N_GQ) * 128, lane < 3, T / N_GQ + 1);
}
template <int N> __device__ __forceinline__ void chain_load_tiles(const Params& p, int h, int z, int seg, int j0, int lane_) {
  const int lane = opaque(lane_);
  const unsigned char* base = p.ws + WS_QKV + (size_t)seg * 32 * 24576 + h * 256;
  const unsigned tb = L_TILE + (seg & 1) * TILE_B;
#pragma unroll
  for (int i = 0; i < N; ++i) {
    const int j = j0 + i;
    unsigned vo;
    if (j < 16) { const unsigned row = 4 * (j & 7) + (lane >> 4); vo = row * 24576u + (j < 8 ? 8192u : 0u) + (((lane & 15) ^ (row & 15)) << 4); }
    else { const unsigned row = 8 * (j - 16) + (lane >> 3); vo = row * 24576u + 16384u + z * 128u + (((lane & 7) ^ (((row >> 1) & 3) << 1)) << 4); }
    glds16c(base, vo, UNI(tb + j * 1024));
  }
}

__device__ __forceinline__ void chain_token_attn(lptr lds, int seg, int qt, int lane_) {
  const int lane = opaque(lane_);
  lptr tb = lds + L_TILE + (seg & 1) * TILE_B;
  const int i = lane & 15, g = lane >> 4;
  f32x4 s0 = {0, 0, 0, 0}, s1 = s0, s2 = s0;
  {
    u32x4 bq[4], am[4], a1[4], a2[4];
    lptr kim = lds + L_KIM + (2 * (i >> 2) + (i & 1)) * 272 + g * 16;
#pragma unroll
    for (int e = 0; e < 4; ++e) {
      const unsigned o = i * 256 + (((4 * e + g) ^ i) << 4);
      bq[e] = lds_ld<u32x4>(tb + T_Q + qt * 4096 + o);
      am[e] = lds_ld<u32x4>(kim + e * 64);
      a1[e] = lds_ld<u32x4>(tb + T_K + o);
      if (qt) a2[e] = lds_ld<u32x4>(tb + T_K + 4096 + o);
    }
    SCHED();
#pragma unroll
    for (int e = 0; e < 4; ++e) { s0 = mfma16(am[e], bq[e], s0); s1 = mfma16(a1[e], bq[e], s1); if (qt) s2 = mfma16(a2[e], bq[e], s2); }
  }
  const int q = i >> 2, pp = lane & 3;
  u32x2 va[4][2]; unsigned vm[4];
#pragma unroll
  for (int t = 0; t < 4; ++t) {
    const unsigned vr = 4 * g + q, vo = vr * 128 + (((2 * t + (pp >> 1)) ^ (((vr >> 1) & 3) << 1)) << 4) + 8 * (pp & 1);
    va[t][0] = lds_tr(tb + T_V + vo); va[t][1] = lds_tr(tb + T_V + 16 * 128 + vo);
    vm[t] = lds_ld<unsigned>(lds + L_VIM + t * 256 + lane * 4);
  }
  SCHED();
  const int qi = 16 * qt + i;
  float v[10];
  v[0] = rbf(rbf(s0[0]) * SCALE); v[1] = rbf(rbf(s0[1]) * SCALE);
#pragma unroll
  for (int r = 0; r < 4; ++r) {
    v[2 + r] = (4 * g + r <= qi) ? rbf(rbf(s1[r]) * SCALE) : -1e9f;
    v[6 + r] = (qt && 16 + 4 * g + r <= qi) ? rbf(rbf(s2[r]) * SCALE) : -1e9f;
  }
  float mx = fmax2(fmax2(fmax2(v[0], v[1]), fmax2(v[2], v[3])), fmax2(fmax2(fmax2(v[4], v[5]), fmax2(v[6], v[7])), fmax2(v[8], v[9])));
  mx = xmax32(xmax16(mx));
  float sum = 0.f;
#pragma unroll
  for (int r = 0; r < 10; ++r) { v[r] = __builtin_amdgcn_exp2f((v[r] - mx) * LOG2E); sum += v[r]; }
  sum = xr32(xr16(sum, false), false);
  const float inv = 1.0f / sum;
  const u32x4 pt = {pk2(v[2] * inv, v[3] * inv), pk2(v[4] * inv, v[5] * inv), pk2(v[6] * inv, v[7] * inv), pk2(v[8] * inv, v[9] * inv)};
  const u32x4 pm = {pk2(v[0] * inv, v[1] * inv), 0u, 0u, 0u};
  lptr ob = lds + L_OXS + qt * 2304;
#pragma unroll
  for (int t = 0; t < 4; ++t) {
    f32x4 o = mfma16(u32x4{va[t][0].x, va[t][0].y, va[t][1].x, va[t][1].y}, pt, f32x4{0, 0, 0, 0});
    o = mfma16(u32x4{vm[t], 0u, 0u, 0u}, pm, o);
    lds_st(ob + i * 144 + (16 * t + 4 * g) * 2, u32x2{pk2(o[0], o[1]), pk2(o[2], o[3])});
  }
}
__device__ __forceinline__ void chain_store_ox(rsrc_t rW, lptr lds, int h, int z, int seg, int qt, int lane_) {
  const int lane = opaque(lane_);
  lptr ob = lds + L_OXS + qt * 2304;
  const unsigned so = UNI(WS_OX + ((unsigned)seg * 32u + 16 * qt) * 8192u + h * 256u + z * 128u);
#pragma unroll
  for (int j = 0; j < 2; ++j) {
    const unsigned row = 8 * j + (lane >> 3);
    const u32x4 w = lds_ld<u32x4>(ob + row * 144 + (lane & 7) * 16);
    st16c(rW, row * 8192u + (lane & 7) * 16, so, w);
  }
}
__device__ __forceinline__ void chain_publish_ox(rsrc_t rW, lptr lds, int c, unsigned done, int lane) {
  WAIT_VM0();
  unsigned old = 0;
  if (lane == 0) old = __hip_atomic_fetch_add((LAS unsigned*)(lds + L_CNT), 1u, __ATOMIC_RELAXED, __HIP_MEMORY_SCOPE_WORKGROUP);
  old = __builtin_amdgcn_readfirstlane(old);
  if (old & 1u) st4c(rW, lane == 0 ? WS_PROG + (R_CHAIN + c) * 128 : 0x80000000u, 0, done);
}

#define GATHER_DBG
#define GATHER_DBG_ARGS
__device__ __forceinline__ void chain_gather(const Params& p, rsrc_t rW, lptr lds, int s, int kb0, bool three, unsigned gnext, int lane_ GATHER_DBG_ARGS) {
  const unsigned g0 = UNI(gnext + (kb0 >> 3) * P_UNIT + (kb0 & 7) * 1024), g1 = UNI(gnext + ((kb0 + 1) >> 3) * P_UNIT + ((kb0 + 1) & 7) * 1024);
  const int lane = opaque(lane_);
  const unsigned lo16 = lane * 16;
  for (unsigned sp = 0; sp < SPIN_CAP * 16u; ++sp)
    if (*(volatile LAS unsigned*)(lds + L_CNT + 8) > (unsigned)s) break;
  ACLK(tfl); ACC(1, tfl, tb2);
  LST(6);
  for (unsigned i = 0; i < p.tune1; ++i) __builtin_amdgcn_s_sleep(1);
  u32x4 v0, v1, v2; unsigned gslot;
  for (unsigned sp = 0;; ++sp) {
    MEMBAR();
    v0 = ld16c(rW, lo16, g0); v1 = ld16c(rW, lo16, g1);
    v2 = ld16c(rW, three ? lo16 + 2048 : 0x80000000u, g0);
    gslot = ((lane & 3) * 16 + 4 * (lane >> 4) + 2 * ((lane >> 3) & 1) + ((lane >> 2) & 1)) * 16;
    SCHED();
    const unsigned mx = max(max(umax4(v0), umax4(v1)), umax4(v2));
    if (__builtin_expect(__all(mx != EMPTY), 1)) break;
    if ((sp & 63u) == 63u) {
      if (__builtin_amdgcn_readfirstlane(ld4c(rW, WS_ABORT)) != 0u) break;
      if (sp > SPIN_CAP) { st4c(rW, WS_ABORT, 0, 3u); break; }
    }
  }
  ACLK(tgn); ACC(2, tgn, tb2);
  LST(7);
  lptr ad = lds + L_AIM + kb0 * 1024 + gslot;
  lds_st(ad, v0); lds_st(ad + 1024, v1);
  if (three) lds_st(ad + 2048, v2);
}

template <bool AW>
__device__ __forceinline__ void chain_path(const Params& p, rsrc_t rW, lptr lds, int c, unsigned exoff) {
  const int tid = threadIdx.x, lane = tid & 63, wave = __builtin_amdgcn_readfirstlane(tid >> 6);
  const int h = c >> 1, z = c & 1;
  constexpr int NV = AW ? 2 : 5, V0 = AW ? 0 : 2;
  u32x8 Wq[9], Wk[9], Wv[NV];
  if (wave == 2) STAMP_AT(c, 0);
  {
    const u16* wo = p.wo + (size_t)(h * 128 + z * 64) * 576;
#pragma unroll
    for (int j = 0; j < 9; ++j) glds16(wo + (wave * 9 + j) * 512 + lane * 8, L_WO + (wave * 9 + j) * 1024);
    chain_stage_w(p, h, z, 0, L_STG, wave, lane);
    chain_stage_w(p, h, z, 1, L_STG + 40960, wave, lane);
    asm volatile("s_waitcnt vmcnt(10)" ::: "memory");
    WG_BAR();
    u32x2 fr[5][4];
    const int gb = lane >> 4, q = (lane & 15) >> 2, pp = lane & 3;
#pragma unroll
    for (int j = 0; j < 5; ++j) {
      const int f = min(wave + 8 * j, 35);
#pragma unroll
      for (int a = 0; a < 4; ++a) {
        const int e = 16 * (gb >> 1) + 8 * (q >> 1) + 4 * (gb & 1) + 2 * (a & 1) + (q & 1) + 32 * (a >> 1);
        fr[j][a] = lds_tr(lds + L_WO + e * 1152 + (16 * f + 4 * pp) * 2);
      }
    }
    WG_BAR();
#pragma unroll
    for (int j = 0; j < 5; ++j) {
      const int f = wave + 8 * j;
      if (f < 36) {
        lds_st(lds + L_WO + f * 2048 + lane * 16, u32x4{fr[j][0].x, fr[j][0].y, fr[j][1].x, fr[j][1].y});
        lds_st(lds + L_WO + f * 2048 + 1024 + lane * 16, u32x4{fr[j][2].x, fr[j][2].y, fr[j][3].x, fr[j][3].y});
      }
    }
    u32x8 tv[2];
#pragma unroll
    for (int kb = 0; kb < 9; ++kb) {
      if (kb < 8) asm volatile("s_waitcnt vmcnt(5)" ::: "memory"); else asm volatile("s_waitcnt vmcnt(0)" ::: "memory");
      WG_BAR();
      lptr img = lds + L_STG + (kb & 1) * 40960;
      Wq[kb] = chain_read_w<256>(img, wave, lane);
      Wk[kb] = chain_read_w<256>(img + 16384, wave, lane);
      if (kb >= V0 && kb < V0 + NV) Wv[kb - V0] = chain_read_w<128>(img + 32768, wave & 3, lane);
      if (!AW && kb >= 7) tv[kb - 7] = chain_read_w<128>(img + 32768, wave & 3, lane);
      WG_BAR();
      if (kb + 2 < 9) chain_stage_w(p, h, z, kb + 2, L_STG + (kb & 1) * 40960, wave, lane);
    }
    if (!AW) {
#pragma unroll
      for (int j = 0; j < 2; ++j) {
        lds_st(lds + L_WV + ((wave & 3) * 2 + j) * 2048 + lane * 16, u32x4{tv[j][0], tv[j][1], tv[j][2], tv[j][3]});
        lds_st(lds + L_WV + ((wave & 3) * 2 + j) * 2048 + 1024 + lane * 16, u32x4{tv[j][4], tv[j][5], tv[j][6], tv[j][7]});
      }
    }
  }
  if (wave == 2) STAMP_AT(c, 1);
  const int myu = wave == 4 ? c : (wave == 5 && c < 8 ? 64 + c : -1);
  if (c < 4 && wave == 4) {
    const unsigned a = UNI(exoff + (c >> 1) * 2097152u + (c & 1) * 8192u);
    unsigned best = 0xffffffffu;
    for (int r = 0; r < 8; ++r) {
      const unsigned long long t0 = clk();
      st4c(rW, lane * 4 + (r & 3) * 256u, a, EMPTY);
      WAIT_VM0();
      best = min(best, (unsigned)(clk() - t0));
    }
    st4c(rW, lane == 0 ? WS_PAR + c * 4 : 0x80000000u, 0, best | 1u);
  }
  if (tid == 0) { lds_st(lds + L_CNT, 0u); lds_st(lds + L_CNT + 8, 0u); }
  WG_BAR();
  wait_ge(rW, WS_PAR + (lane & 3) * 4, true, 1u);
  unsigned pw;
  {
    const u32x4 b = ld16c(rW, WS_PAR);
    pw = UNI((b.y < b.x ? 1u : 0u) | (b.w < b.z ? 2u : 0u));
    if (p.tune2 & 0x100u) pw = (p.tune2 >> 4) & 3u;
  }
  const unsigned pA = UNI(exoff + (pw & 1u) * 8192u), pB = UNI(exoff + 2097152u + ((pw >> 1) & 1u) * 8192u);
  const unsigned Gb = pA + 72u * P_UNIT;
  const unsigned go = myu >= 0 ? ((unsigned)myu >> 6) * P_UNIT + (myu & 63) * 128u : 0u;
  if (!AW) {
    const u32x4 e4 = {EMPTY, EMPTY, EMPTY, EMPTY};
    if (myu >= 0) {
#pragma unroll
      for (int j = 0; j < 8; ++j) { st16c(rW, j * 1024 + lane * 16, UNI(pA + myu * P_UNIT), e4); st16c(rW, j * 1024 + lane * 16, UNI(pB + myu * P_UNIT), e4); }
      st16c(rW, lane < 24 ? (lane >> 3) * 2 * P_UNIT + (lane & 7) * 16 : 0x80000000u, UNI(Gb + go), e4);
    }
    WAIT_VM0();
  }
  if (wave >= 6) {
#pragma unroll
    for (int j = 0; j < 5; ++j) {
      const int kb = (wave - 6) + 2 * j;
      if (kb < 9) {
        const int g = lane >> 4, nh = (lane >> 3) & 1, par = (lane >> 2) & 1, jj = lane & 3;
        const u16* src = p.mem0 + (2 * g + par) * 576 + 64 * kb + 8 * nh + 2 * jj;
        unsigned e[8];
#pragma unroll
        for (int t = 0; t < 8; ++t) e[t] = *(const GAS u16*)(src + 16 * (t & 3) + (t >> 2));
        lds_st(lds + L_AIM + kb * 1024 + (jj * 16 + 4 * g + 2 * nh + par) * 16, u32x4{e[0] | (e[1] << 16), e[2] | (e[3] << 16), e[4] | (e[5] << 16), e[6] | (e[7] << 16)});
      }
    }
  }
  chain_wait_tiles(rW, h, 0, lane);
  chain_load_tiles<AW ? 2 : 3>(p, h, z, 0, AW ? 2 * wave : 8 + 3 * (wave - 4), lane);
  WAIT_VM0();
  WG_BAR();
  if (wave == 4) {
    if (lane == 0) __hip_atomic_fetch_add((GAS unsigned*)(p.ws + WS_CBAR), 1u, __ATOMIC_RELAXED, __HIP_MEMORY_SCOPE_AGENT);
    wait_ge(rW, WS_CBAR, true, 64u);
  }
  const int idx = ((lane >> 1) & 1) ? 0xEEEE : 0x4444;
  const int i16 = lane & 15, g = lane >> 4;
  lptr aim = lds + L_AIM + lane * 16;
  lptr wof = lds + L_WO + lane * 16;
  const u32x4 rs4 = {UNI((unsigned)(unsigned long long)p.ws), UNI((unsigned)((unsigned long long)p.ws >> 32)), UNI((unsigned)(p.wsbytes > 0xFFFFFFF0ull ? 0xFFFFFFF0ull : p.wsbytes)), 0x00020000u};
  const unsigned vtro = (4 * g + (i16 >> 2)) * 128 + (((2 * wave + ((lane & 3) >> 1)) ^ ((((4 * g + (i16 >> 2)) >> 1) & 3) << 1)) << 4) + 8 * (lane & 1);
  unsigned g3prev = 2, g3next = 1;
  const unsigned pvo = (lane >> 3) * P_UNIT + (lane & 7) * 16;
  WG_BAR();

  for (int s = 0; s < NSEG; ++s) {
    ACLK(tb0);
    LST(0);
    STAMP_CLK(0);
    f32x4 aq = {0, 0, 0, 0}, ak = aq, av = aq;
    u32x4 kx[AW ? 8 : 1];
    constexpr int RG = AW ? 4 : 5;
    u32x4 a[RG];
    u32x8 x0;
    {
#pragma unroll
      for (int kb = 0; kb < RG; ++kb) a[kb] = lds_ld<u32x4>(aim + kb * 1024);
      SCHED();
      if (s + 1 < NSEG && !(p.tune2 & 2u)) chain_load_tiles<AW ? 2 : 3>(p, h, z, s + 1, AW ? 2 * wave : 8 + 3 * (wave - 4), lane);
      if (!AW && wave >= 6 && s > 0) chain_store_ox(rW, lds, h, z, s - 1, wave - 6, lane);
      SCHED();
      lptr tb = lds + L_TILE + (s & 1) * TILE_B + T_K + i16 * 256;
      const int i16o = opaque(i16);
#pragma unroll
      for (int kb = 0; kb < 9; ++kb) {
        aq = smfmac(a[kb % RG], Wq[kb], aq, idx);
        ak = smfmac(a[kb % RG], Wk[kb], ak, idx);
        if (AW && kb < 2) av = smfmac(a[kb % RG], Wv[kb], av, idx);
        if (kb + RG < 9) a[kb % RG] = lds_ld<u32x4>(aim + (kb + RG) * 1024);
        if (AW && kb >= 5) { const int e = kb - 5; const unsigned o = ((4 * e + g) ^ i16o) << 4; kx[2 * e] = lds_ld<u32x4>(tb + o); kx[2 * e + 1] = lds_ld<u32x4>(tb + 4096 + o); }
        if (!AW && kb == 4) x0 = chain_wo_frag(lds + L_WV + ((wave & 3) * 2) * 2048 + lane * 16);
        SCHED();
      }
    }
    {
      const unsigned qp = pk2(aq[0] + aq[2], aq[1] + aq[3]), kp = pk2(ak[0] + ak[2], ak[1] + ak[3]);
      lptr qw = lds + L_QIM + 2 * g * 272 + (16 * wave + i16) * 2;
      lds_st(qw, (u16)qp); lds_st(qw + 272, (u16)(qp >> 16));
      lds_st(qw + (L_KIM - L_QIM), (u16)kp); lds_st(qw + (L_KIM - L_QIM) + 272, (u16)(kp >> 16));
    }
    WG_BAR();
    if (AW) __builtin_amdgcn_s_setprio(1);
    const unsigned pb = UNI(((s & 1) ? pB : pA) + c * 128);
    if (!AW) {
      {
        const u32x4 b2 = lds_ld<u32x4>(aim + 2 * 1024), b3 = lds_ld<u32x4>(aim + 3 * 1024);
        const u32x8 x1 = chain_wo_frag(lds + L_WV + ((wave & 3) * 2 + 1) * 2048 + lane * 16);
        SCHED();
        av = smfmac(a[4 % RG], Wv[2], av, idx);
        av = smfmac(a[0], Wv[3], av, idx);
        av = smfmac(a[1], Wv[4], av, idx);
        av = smfmac(a[2], x0, av, idx);
        av = smfmac(b2, Wv[0], av, idx);
        av = smfmac(b3, Wv[1], av, idx);
        av = smfmac(a[3], x1, av, idx);
      }
      lds_st(lds + L_VPX + (wave - 4) * 512 + lane * 8, f32x2{av[0] + av[2], av[1] + av[3]});
      WG_BAR();
      const u32x8 w0 = chain_wo_frag(wof + (wave * 4 + 0) * 2048), w1 = chain_wo_frag(wof + (wave * 4 + 1) * 2048),
                  w2 = chain_wo_frag(wof + (wave * 4 + 2) * 2048), w3 = chain_wo_frag(wof + (wave * 4 + 3) * 2048);
      f32x4 d0 = {0, 0, 0, 0}, d1 = d0;
      asm volatile("" : "+v"(d0), "+v"(d1));
      if (wave >= 6 && s > 0) chain_publish_ox(rW, lds, c, (unsigned)s, lane);
      if (myu >= 0 && s > 0) {
        const unsigned em = (unsigned)opaque(-1), lo = opaque(lane) * 16;
        const u32x4 e4 = {em, em, em, em};
        const unsigned pu = UNI((((s - 1) & 1) ? pB : pA) + myu * P_UNIT), gc = UNI(Gb + g3prev * 2 * P_UNIT + go);
        if (!(p.tune2 & 4u)) {
#pragma unroll
        for (int j = 0; j < 8; ++j) st16c(rW, lo + (j & 3) * 1024, pu + (j >> 2) * 4096, e4);
        }
        st16c(rW, lo < 128 ? lo : 0x80000000u, gc, e4);
      }
      const unsigned pu = UNI(((s & 1) ? pB : pA) + myu * P_UNIT), pu4 = UNI(pu + 4096);
      const unsigned gnext = Gb + g3next * 2 * P_UNIT;
      const unsigned gw = UNI(gnext + go);
      SCHED();
      WG_BAR();
      __builtin_amdgcn_s_setprio(2);
      LST(1);
      if (s + 1 < NSEG) {
        {
          const u32x4 oa = lds_ld<u32x4>(lds + L_OIM + lane * 16);
          d0 = smfmac(oa, w0, d0, idx);
          d1 = smfmac(oa, w1, d1, idx);
          const f32x4 d2 = smfmac(oa, w2, f32x4{0, 0, 0, 0}, idx), d3 = smfmac(oa, w3, f32x4{0, 0, 0, 0}, idx);
          st16c(rW, pvo, pb + 8 * wave * P_UNIT, u32x4{chain_pk_tile(d0), chain_pk_tile(d1), chain_pk_tile(d2), chain_pk_tile(d3)});
        }
        if (myu >= 0) {
          for (unsigned i = 0; i < p.tune0; ++i) __builtin_amdgcn_s_sleep(1);
          ACLK(tpi); ACC(5, tpi, tb2);
          LST(2);
          f32x4 a0; unsigned gvo;
          const unsigned lo16 = opaque(lane) * 16;
          for (unsigned sp = 0;; ++sp) {
            MEMBAR();
            u32x4 v[8];
#pragma unroll
            for (int j = 0; j < 8; ++j) v[j] = ld16c(rW, lo16 + (j & 3) * 1024, j < 4 ? pu : pu4);
            u32x4 sel;
            {
              const int l = opaque(lane), i = l & 15, jj = 2 * (i & 3) + ((i >> 2) & 1);
              const unsigned one = i < 8 ? ((jj & 1) ? 0x3F800000u : 0x00003F80u) : 0u; const int w = jj >> 1;
              sel = u32x4{w == 0 ? one : 0u, w == 1 ? one : 0u, w == 2 ? one : 0u, w == 3 ? one : 0u};
              gvo = (l < 32 && i < 8) ? (l >> 4) * 64 + i * 8 : 0x80000000u;
            }
            SCHED();
            a0 = mfma16(sel, v[0], f32x4{0, 0, 0, 0});
#pragma unroll
            for (int j = 1; j < 8; ++j) a0 = mfma16(sel, v[j], a0);
            if (__builtin_expect(!__any(a0[0] != a0[0]), 1)) break;
            asm volatile("" : "+v"(v[0]), "+v"(v[1]), "+v"(v[2]), "+v"(v[3]), "+v"(v[4]), "+v"(v[5]), "+v"(v[6]), "+v"(v[7]));
            unsigned mx = 0;
#pragma unroll
            for (int j = 0; j < 8; ++j) mx = max(mx, umax4(v[j]));
            if (__all(mx != EMPTY)) break;
            if ((sp & 63u) == 63u) {
              if (__builtin_amdgcn_readfirstlane(ld4c(rW, WS_ABORT)) != 0u) break;
              if (sp > SPIN_CAP) { st4c(rW, WS_ABORT, 0, 2u); break; }
            }
          }
          ACLK(tps); ACC(1, tps, tb2);
          LST(3);
#pragma unroll
          for (int r = 0; r < 4; ++r) a0[r] += dppf<0x128>(a0[r]);
          const u32x2 o = {min(pk2(a0[0], a0[1]), 0xFFFEFFFFu), min(pk2(a0[2], a0[3]), 0xFFFEFFFFu)};
          st8c(rW, gvo, gw, o);
          if (wave == 4) lds_st(lds + L_CNT + 8, (unsigned)s + 1u);
          ACLK(tgs); ACC(2, tgs, tb2);
          LST(4);
        }
        if (wave >= 6) chain_gather(p, rW, lds, s, wave == 6 ? 0 : 3, wave == 6, Gb + g3next * 2 * P_UNIT, lane GATHER_DBG);
      }
    } else {
      f32x4 s0 = {0, 0, 0, 0}, s1 = s0, s2 = s0;
      {
        lptr qb = lds + L_QIM + (i16 & 7) * 272 + g * 16, kb_ = lds + L_KIM + (2 * (i16 >> 2) + (i16 & 1)) * 272 + g * 16;
        u32x4 bq[4], am[4];
#pragma unroll
        for (int e = 0; e < 4; ++e) bq[e] = lds_ld<u32x4>(qb + e * 64);
        am[0] = lds_ld<u32x4>(kb_); am[1] = lds_ld<u32x4>(kb_ + 64);
        SCHED();
        s1 = mfma16(kx[0], bq[0], s1); s2 = mfma16(kx[1], bq[0], s2);
        s1 = mfma16(kx[2], bq[1], s1); s2 = mfma16(kx[3], bq[1], s2);
        am[2] = lds_ld<u32x4>(kb_ + 128); am[3] = lds_ld<u32x4>(kb_ + 192);
        SCHED();
        s0 = mfma16(am[0], bq[0], s0); s1 = mfma16(kx[4], bq[2], s1); s2 = mfma16(kx[5], bq[2], s2);
        s0 = mfma16(am[1], bq[1], s0); s1 = mfma16(kx[6], bq[3], s1); s2 = mfma16(kx[7], bq[3], s2);
        s0 = mfma16(am[2], bq[2], s0); s0 = mfma16(am[3], bq[3], s0);
      }
      STAMP_CLK(3);
      lptr tv = lds + L_TILE + (s & 1) * TILE_B + T_V + vtro;
      const u32x2 va0 = lds_tr(tv), va1 = lds_tr(tv + 16 * 128);
      const u32x8 w0 = chain_wo_frag(wof + (wave * 4 + 0) * 2048), w1 = chain_wo_frag(wof + (wave * 4 + 1) * 2048),
                  w2 = chain_wo_frag(wof + (wave * 4 + 2) * 2048), w3 = chain_wo_frag(wof + (wave * 4 + 3) * 2048);
      SCHED();
      float v[10] = {s0[0], s0[1], s1[0], s1[1], s1[2], s1[3], s2[0], s2[1], s2[2], s2[3]};
      float mx = fmax2(fmax2(fmax2(v[0], v[1]), fmax2(v[2], v[3])), fmax2(fmax2(fmax2(v[4], v[5]), fmax2(v[6], v[7])), fmax2(v[8], v[9])));
      mx = xmax32(xmax16(mx));
      const float nm = -mx * (SCALE * LOG2E);
#pragma unroll
      for (int r = 0; r < 10; ++r) v[r] = __builtin_amdgcn_exp2f(__builtin_fmaf(v[r], SCALE * LOG2E, nm));
      const u32x4 pt = {pk2(v[2], v[3]), pk2(v[4], v[5]), pk2(v[6], v[7]), pk2(v[8], v[9])};
      const u32x4 pm = {pk2(v[0], v[1]), 0u, 0u, 0u};
      SCHED();
      RAW_BAR();
      const f32x2 vp = lds_ld<f32x2>(lds + L_VPX + wave * 512 + lane * 8);
      SCHED();
      f32x4 o = mfma16(u32x4{va0.x, va0.y, va1.x, va1.y}, pt, f32x4{0, 0, 0, 0});
      float sum = ((v[0] + v[1]) + (v[2] + v[3])) + ((v[4] + v[5]) + (v[6] + v[7])) + (v[8] + v[9]);
      sum = xr32(xr16(sum, false), false);
      const float inv = __builtin_amdgcn_rcpf(sum);
      SCHED();
      const unsigned vm = pk2(av[0] + av[2] + vp[0], av[1] + av[3] + vp[1]);
      o = mfma16(u32x4{vm, 0u, 0u, 0u}, pm, o);
      lds_st(lds + L_VIM + wave * 256 + lane * 4, vm);
      if (i16 < 8) lds_st(lds + L_OIM + (wave * 16 + 4 * (i16 >> 1) + 2 * (g >> 1) + (i16 & 1)) * 16 + (g & 1) * 8, u32x2{pk2(o[0] * inv, o[1] * inv), pk2(o[2] * inv, o[3] * inv)});
      STAMP_CLK(5);
      WG_BAR();
      LST(1);
      STAMP_CLK(6);
      if (s + 1 < NSEG) {
        const u32x4 oa = lds_ld<u32x4>(lds + L_OIM + lane * 16);
        const f32x4 z4 = {0, 0, 0, 0};
        if (wave == 0) {
          const u32x8 x0 = chain_wo_frag(wof + 32 * 2048), x1 = chain_wo_frag(wof + 33 * 2048);
          SCHED();
          const f32x4 d0 = smfmac(oa, w0, z4, idx), d1 = smfmac(oa, w1, z4, idx), d2 = smfmac(oa, w2, z4, idx), d3 = smfmac(oa, w3, z4, idx);
          SCHED();
          const u32x8 x2 = chain_wo_frag(wof + 34 * 2048), x3 = chain_wo_frag(wof + 35 * 2048);
          st16c(rW, pvo, pb, u32x4{chain_pk_tile(d0), chain_pk_tile(d1), chain_pk_tile(d2), chain_pk_tile(d3)});
          const f32x4 e0 = smfmac(oa, x0, z4, idx), e1 = smfmac(oa, x1, z4, idx), e2 = smfmac(oa, x2, z4, idx), e3 = smfmac(oa, x3, z4, idx);
          st16c(rW, pvo, pb + 64 * P_UNIT, u32x4{chain_pk_tile(e0), chain_pk_tile(e1), chain_pk_tile(e2), chain_pk_tile(e3)});
        } else {
          const f32x4 d0 = smfmac(oa, w0, z4, idx), d1 = smfmac(oa, w1, z4, idx), d2 = smfmac(oa, w2, z4, idx), d3 = smfmac(oa, w3, z4, idx);
          st16c(rW, pvo, pb + 8 * wave * P_UNIT, u32x4{chain_pk_tile(d0), chain_pk_tile(d1), chain_pk_tile(d2), chain_pk_tile(d3)});
        }
      }
      LST(2);
      if (wave < 2) __builtin_amdgcn_s_setprio(0); else __builtin_amdgcn_s_setprio(2);
      if (wave < 2) {
        if (!(p.tune2 & 1u)) chain_token_attn(lds, s, wave, lane);
        LST(3);
        if (s + 1 == NSEG) { asm volatile("s_waitcnt lgkmcnt(0)" ::: "memory"); chain_store_ox(rW, lds, h, z, s, wave, lane); chain_publish_ox(rW, lds, c, NSEG, lane); }
      } else if (s + 1 < NSEG) {
        if (s + 2 < NSEG && ((s + 2) & 3) == 0 && wave == 2) chain_wait_tiles(rW, h, s + 2, lane);
        chain_gather(p, rW, lds, s, wave == 2 ? 5 : 7, false, Gb + g3next * 2 * P_UNIT, lane GATHER_DBG);
      }
    }
    LST(5);
    if ((!AW && wave == 5 && myu < 0) || (AW && wave < 2)) WAIT_VM0();
    g3prev = g3prev == 2 ? 0 : g3prev + 1; g3next = g3next == 2 ? 0 : g3next + 1;
    WG_BAR();
    __builtin_amdgcn_s_setprio(0);
    STAMP_CLK(9);
  }
  if (wave == 2) STAMP_AT(c, 66);
}

__device__ __forceinline__ void publish_wg(rsrc_t rW, int role, unsigned val) {
  WAIT_VM0();
  WG_BAR();
  if (threadIdx.x == 0) st4c(rW, WS_PROG + role * 128, 0, val);
}

__device__ __forceinline__ void t4_role(const Params& p, rsrc_t rW, lptr lds, int a) {
  const int tid = threadIdx.x, lane = tid & 63, wave = __builtin_amdgcn_readfirstlane(tid >> 6);
  const int ng = a % 9, kq = a / 9, nt = wave & 1, kr = wave >> 1;
  u32x4 W[16];
  {
    const u16* src = p.wo + (size_t)(kq * 1024 + (lane >> 3)) * 576 + ng * 64 + (lane & 7) * 8;
#pragma unroll
    for (int j = 0; j < 16; ++j) glds16(src + (size_t)(wave * 16 + j) * 8 * 576, (wave * 16 + j) * 1024);
    WAIT_VM0();
    WG_BAR();
    const int G = lane >> 4, q = (lane & 15) >> 2, pp = lane & 3;
#pragma unroll
    for (int ks = 0; ks < 16; ++ks) {
      const int row = kr * 256 + 16 * ks + 8 * (G >> 1) + q, col = nt * 32 + 16 * (G & 1) + 4 * pp;
      const u32x2 a0 = lds_tr(lds + row * 128 + col * 2), a1 = lds_tr(lds + (row + 4) * 128 + col * 2);
      W[ks] = u32x4{a0.x, a0.y, a1.x, a1.y};
    }
    WG_BAR();
  }
  for (int s = 0; s < NSEG; ++s) {
    if (wave == 0) wait_ge(rW, WS_PROG + (R_CHAIN + kq * 16 + (lane & 15)) * 128, true, (unsigned)s + 1u);
    WG_BAR();
    {
      const unsigned char* base = p.ws + WS_OX + (size_t)s * 32 * 8192 + kq * 2048;
#pragma unroll
      for (int j = 0; j < 8; ++j) {
        const unsigned id = wave * 8 + j, row = id >> 1, ch = (id & 1) * 64 + lane;
        glds16c(base, row * 8192u + ((ch ^ (row & 15)) << 4), UNI(id * 1024));
      }
      WAIT_VM0();
      WG_BAR();
    }
    f32x16 acc;
#pragma unroll
    for (int r = 0; r < 16; ++r) acc[r] = 0.f;
    {
      const unsigned row = lane & 31, hh = lane >> 5;
#pragma unroll
      for (int ks = 0; ks < 16; ++ks) {
        const unsigned ch = kr * 32 + 2 * ks + hh;
        acc = mfma32(W[ks], lds_ld<u32x4>(lds + row * 2048 + ((ch ^ (row & 15)) << 4)), acc);
      }
      lptr red = lds + 65536 + (kr * 2 + nt) * 4608 + row * 144 + hh * 16;
#pragma unroll
      for (int g = 0; g < 4; ++g) lds_st(red + g * 32, f32x4{acc[4 * g], acc[4 * g + 1], acc[4 * g + 2], acc[4 * g + 3]});
    }
    WG_BAR();
    {
      const unsigned tok = tid >> 4, c4 = tid & 15;
      lptr red = lds + 65536 + (c4 >> 3) * 4608 + tok * 144 + (c4 & 7) * 16;
      const f32x4 v = (lds_ld<f32x4>(red) + lds_ld<f32x4>(red + 2 * 4608)) + (lds_ld<f32x4>(red + 4 * 4608) + lds_ld<f32x4>(red + 6 * 4608));
      st16c(rW, tok * 2304u + ng * 256u + c4 * 16, UNI(WS_PT + (unsigned)(s * 4 + kq) * 73728u), __builtin_bit_cast(u32x4, v));
    }
    publish_wg(rW, R_T4 + a, (unsigned)s + 1u);
  }
}

__device__ __forceinline__ void ln_role(const Params& p, rsrc_t rW, lptr lds, int r8) {
  const int tid = threadIdx.x, lane = tid & 63, wave = __builtin_amdgcn_readfirstlane(tid >> 6);
  const float* mod = (const float*)(p.ws + WS_MOD);
  if (wave >= 4) return;
  const bool two = lane < 8;
  float ga[16];
#pragma unroll
  for (int j = 0; j < 16; ++j) ga[j] = mod[2 * 576 + (j < 8 ? 8 * lane : 512 + 8 * (lane & 7)) + (j & 7)];
  for (int s = 0; s < NSEG; ++s) {
    const int row = s * 32 + r8 * 4 + wave;
    const u32x4 xa = *(const GAS u32x4*)(p.x + (size_t)row * 576 + 8 * lane);
    const u32x4 xb = *(const GAS u32x4*)(p.x + (size_t)row * 576 + 512 + 8 * (lane & 7));
    wait_ge(rW, WS_PROG + (R_T4 + (lane < 36 ? lane : 0)) * 128, lane < 36, (unsigned)s + 1u);
    f32x4 t[4][4];
    const unsigned so = UNI(WS_PT + (unsigned)s * 4u * 73728u + (r8 * 4 + wave) * 2304u);
#pragma unroll
    for (int k = 0; k < 4; ++k) {
      t[k][0] = __builtin_bit_cast(f32x4, ld16c(rW, lane * 32, so + k * 73728u));
      t[k][1] = __builtin_bit_cast(f32x4, ld16c(rW, lane * 32 + 16, so + k * 73728u));
      t[k][2] = __builtin_bit_cast(f32x4, ld16c(rW, two ? 2048 + lane * 32 : 0x80000000u, so + k * 73728u));
      t[k][3] = __builtin_bit_cast(f32x4, ld16c(rW, two ? 2048 + lane * 32 + 16 : 0x80000000u, so + k * 73728u));
    }
    float v[16];
    unpack8(xa, v); unpack8(xb, v + 8);
#pragma unroll
    for (int j = 0; j < 16; ++j) {
      const float att = (t[0][j >> 2][j & 3] + t[1][j >> 2][j & 3]) + (t[2][j >> 2][j & 3] + t[3][j >> 2][j & 3]);
      v[j] = rbf(v[j] + rbf(ga[j] * rbf(att)));
    }
    u16* x1 = (u16*)(p.ws + WS_X1) + (size_t)row * 576;
    *(GAS u32x4*)(x1 + 8 * lane) = u32x4{pk2(v[0], v[1]), pk2(v[2], v[3]), pk2(v[4], v[5]), pk2(v[6], v[7])};
    if (two) *(GAS u32x4*)(x1 + 512 + 8 * lane) = u32x4{pk2(v[8], v[9]), pk2(v[10], v[11]), pk2(v[12], v[13]), pk2(v[14], v[15])};
    u32x4 o0, o1;
    adaln_row(v, lane, mod + 4 * 576, mod + 3 * 576, o0, o1);
    const unsigned ho = UNI(WS_H2 + (unsigned)row * 1152u);
    st16c(rW, lane * 16, ho, o0);
    st16c(rW, two ? 1024 + lane * 16 : 0x80000000u, ho, o1);
    WAIT_VM0();
    st4c(rW, lane == 0 ? WS_LNP + (r8 * 4 + wave) * 128 : 0x80000000u, 0, (unsigned)s + 1u);
  }
}

__device__ __forceinline__ float gelu_tanh(float x) {
  const float u = 0.7978845608028654f * (x + 0.044715f * x * x * x);
  const float e = __builtin_amdgcn_exp2f(u * (2.0f * LOG2E));
  const float th = 1.0f - 2.0f * __builtin_amdgcn_rcpf(e + 1.0f);
  return 0.5f * x * (1.0f + th);
}

__device__ __forceinline__ void t5_role(const Params& p, rsrc_t rW, lptr lds, int b) {
  const int tid = threadIdx.x, lane = tid & 63, wave = __builtin_amdgcn_readfirstlane(tid >> 6);
  u32x4 W[18];
  {
    const u16* src = p.win + (size_t)(lane >> 4) * F_ + b * 128 + (lane & 15) * 8;
#pragma unroll
    for (int j = 0; j < 18; ++j) glds16(src + (size_t)(wave * 18 + j) * 4 * F_, (wave * 18 + j) * 1024);
    WAIT_VM0();
    WG_BAR();
    const int g = lane >> 4, q = (lane & 15) >> 2, pp = lane & 3;
#pragma unroll
    for (int ks = 0; ks < 18; ++ks) {
      const int row = 32 * ks + 8 * g + q, col = wave * 16 + 4 * pp;
      const u32x2 a0 = lds_tr(lds + row * 256 + col * 2), a1 = lds_tr(lds + (row + 4) * 256 + col * 2);
      W[ks] = u32x4{a0.x, a0.y, a1.x, a1.y};
    }
    WG_BAR();
  }
  unsigned so[5];
#pragma unroll
  for (int j = 0; j < 5; ++j) {
    const unsigned L = (wave + 8 * j) * 1024 + lane * 16, row = L / 1152u, ch = (L % 1152u) >> 4;
    so[j] = row * 1152u + ((ch ^ ((row >> 1) & 7)) << 4);
  }
  const int i16 = lane & 15, g = lane >> 4;
  for (int s = 0; s < NSEG; ++s) {
    if (wave == 0) wait_ge(rW, WS_LNP + (lane & 31) * 128, true, (unsigned)s + 1u);
    WG_BAR();
    {
      const unsigned char* base = p.ws + WS_H2 + (size_t)s * 32 * 1152;
#pragma unroll
      for (int j = 0; j < 5; ++j) if (wave + 8 * j < 36) glds16c(base, so[j], UNI((wave + 8 * j) * 1024));
      WAIT_VM0();
      WG_BAR();
    }
    f32x4 acc[2] = {f32x4{0, 0, 0, 0}, f32x4{0, 0, 0, 0}};
#pragma unroll
    for (int tt = 0; tt < 2; ++tt) {
      const unsigned row = 16 * tt + i16, sw = (row >> 1) & 7;
      lptr r0 = lds + row * 1152 + ((g ^ sw) << 4), r1 = lds + row * 1152 + (((g + 4) ^ sw) << 4);
#pragma unroll
      for (int ks = 0; ks < 18; ++ks) acc[tt] = mfma16(W[ks], lds_ld<u32x4>(((ks & 1) ? r1 : r0) + (ks >> 1) * 128), acc[tt]);
    }
    lptr zb = lds + 40960;
#pragma unroll
    for (int tt = 0; tt < 2; ++tt) {
      const unsigned row = 16 * tt + i16, n = wave * 16 + 4 * g;
      float z[4];
#pragma unroll
      for (int r = 0; r < 4; ++r) z[r] = gelu_tanh(rbf(acc[tt][r]));
      lds_st(zb + row * 256 + (((n >> 3) ^ (row & 15)) << 4) + (n & 4) * 2, u32x2{pk2(z[0], z[1]), pk2(z[2], z[3])});
    }
    WG_BAR();
    {
      const unsigned tok = tid >> 4, ch = tid & 15;
      const u32x4 w = lds_ld<u32x4>(zb + tok * 256 + ((ch ^ (tok & 15)) << 4));
      st16c(rW, tok * 9216u + ch * 16, UNI(WS_Z + (unsigned)s * 32u * 9216u + b * 256u), w);
      if (b >= 18) {
        float v[8];
        unpack8(w, v);
        float sum = ((v[0] + v[1]) + (v[2] + v[3])) + ((v[4] + v[5]) + (v[6] + v[7]));
        sum += dppf<0xB1>(sum); sum += dppf<0x4E>(sum); sum += dppf<0x141>(sum); sum += dppf<0x140>(sum);
        const float mean = sum * (1.0f / 128.0f);
        float m2 = 0.f;
#pragma unroll
        for (int j = 0; j < 8; ++j) { const float d = v[j] - mean; m2 += d * d; }
        m2 += dppf<0xB1>(m2); m2 += dppf<0x4E>(m2); m2 += dppf<0x141>(m2); m2 += dppf<0x140>(m2);
        st8c(rW, ch == 0 ? tok * 8u : 0x80000000u, UNI(WS_ZS + (unsigned)(s * 18 + b - 18) * 256u), u32x2{__builtin_bit_cast(unsigned, mean), __builtin_bit_cast(unsigned, m2)});
      }
    }
    publish_wg(rW, R_T5 + b, (unsigned)s + 1u);
  }
}

constexpr unsigned G_WS = 0, G_Z = 32768, G_ZB = 14336, G_ST = 65536;
__device__ __forceinline__ void g_role(const Params& p, rsrc_t rW, lptr lds, int idx_) {
  const int tid = threadIdx.x, lane = tid & 63, wave = __builtin_amdgcn_readfirstlane(tid >> 6);
  const int mt = idx_ / 12, nt = idx_ % 12, t0 = 256 * mt, f0 = 192 * nt, wf = wave & 1, wt = wave >> 1;
  f32x16 acc[3][2];
#pragma unroll
  for (int i = 0; i < 3; ++i)
#pragma unroll
    for (int j = 0; j < 2; ++j)
#pragma unroll
      for (int r = 0; r < 16; ++r) acc[i][j][r] = 0.f;
  const unsigned zrow = tid / 12, zch = (tid % 12) * 2;
  const bool zact = tid < 384;
  const unsigned zvo = zact ? zrow * 9216u + (FH + f0) * 2u + zch * 16u : 0x80000000u;
  const u16* wsrc = p.wsp + (size_t)(t0 + wave * 32 + (lane >> 2)) * S_ + (((lane & 3) ^ ((lane >> 4) & 3)) * 8);
  unsigned known = 0;
  u32x4 z0, z1;
  auto issue = [&](int s) {
    z0 = ld16c(rW, zvo, UNI(WS_Z + (unsigned)s * 32u * 9216u)); z1 = ld16c(rW, zact ? zvo + 16 : 0x80000000u, UNI(WS_Z + (unsigned)s * 32u * 9216u));
    glds16(wsrc + 32 * s, UNI(G_WS + (s & 1) * 16384 + wave * 2048));
    glds16(wsrc + 32 * s + 16 * S_, UNI(G_WS + (s & 1) * 16384 + wave * 2048 + 1024));
    if (wave == 7) {
      const unsigned tok = lane & 31, hf = lane >> 5;
      float m[9], q[9];
#pragma unroll
      for (int j = 0; j < 9; ++j) { const u32x2 v = ld8c(rW, tok * 8 + (hf * 9 + j) * 256, UNI(WS_ZS + (unsigned)s * 18u * 256u)); const unsigned vx = v.x, vy = v.y; m[j] = __builtin_bit_cast(float, vx); q[j] = __builtin_bit_cast(float, vy); }
      float sm = 0.f, sq = 0.f;
#pragma unroll
      for (int j = 0; j < 9; ++j) { sm += m[j]; sq += q[j]; }
      const float mean = xr32(sm, false) * (1.0f / 18.0f);
      float dv = 0.f;
#pragma unroll
      for (int j = 0; j < 9; ++j) { const float d = m[j] - mean; dv += d * d; }
      const float var = (xr32(sq, false) + 128.0f * xr32(dv, false)) * (1.0f / 2304.0f);
      const float sd = rbf(__builtin_sqrtf(rbf(rbf(var) + 1e-5f)));
      if (lane < 32) lds_st(lds + G_ST + (s & 1) * 256 + tok * 8, f32x2{rbf(mean), 1.0f / sd});
    }
  };
  auto finish = [&](int s) {
    if (zact) {
      const f32x2 st = lds_ld<f32x2>(lds + G_ST + (s & 1) * 256 + zrow * 8);
      float v[16];
      unpack8(z0, v); unpack8(z1, v + 8);
#pragma unroll
      for (int j = 0; j < 16; ++j) v[j] = rbf(v[j] - st[0]) * st[1];
      lptr d = lds + G_Z + (s & 1) * G_ZB + zrow * 448 + zch * 16;
      lds_st(d, u32x4{pk2(v[0], v[1]), pk2(v[2], v[3]), pk2(v[4], v[5]), pk2(v[6], v[7])});
      lds_st(d + 16, u32x4{pk2(v[8], v[9]), pk2(v[10], v[11]), pk2(v[12], v[13]), pk2(v[14], v[15])});
    }
  };
  auto wait_seg = [&](int s) {
    if (wave == 0) {
      wait_ge(rW, WS_PROG + (R_T5 + 18 + (lane < 18 ? lane : 0)) * 128, lane < 18, (unsigned)s + 1u);
      unsigned v = ld4c(rW, WS_PROG + (R_T5 + 18 + (lane < 18 ? lane : 0)) * 128);
      v = min(v, (unsigned)__shfl_xor((int)v, 1)); v = min(v, (unsigned)__shfl_xor((int)v, 2)); v = min(v, (unsigned)__shfl_xor((int)v, 4));
      v = min(v, (unsigned)__shfl_xor((int)v, 8)); v = min(v, (unsigned)__shfl_xor((int)v, 16)); v = min(v, (unsigned)__shfl_xor((int)v, 32));
      if (lane == 0) lds_st(lds + G_ST + 512, max(v, (unsigned)s + 1u));
    }
    WG_BAR();
    known = UNI(lds_ld<unsigned>(lds + G_ST + 512));
    WG_BAR();
  };
  const unsigned hh = lane >> 5, G = lane >> 4, q4 = (lane & 15) >> 2, p4 = lane & 3;
  auto compute = [&](int s) {
    lptr wb = lds + G_WS + (s & 1) * 16384, zb = lds + G_Z + (s & 1) * G_ZB;
#pragma unroll
    for (int ks = 0; ks < 2; ++ks) {
      u32x4 bt[2];
#pragma unroll
      for (int j = 0; j < 2; ++j) { const unsigned row = wt * 64 + j * 32 + (lane & 31); bt[j] = lds_ld<u32x4>(wb + row * 64 + (((2 * ks + hh) ^ ((row >> 2) & 3)) << 4)); }
#pragma unroll
      for (int i = 0; i < 3; ++i) {
        lptr a = zb + (16 * ks + 8 * (G >> 1) + q4) * 448 + (wf * 96 + i * 32 + 16 * (G & 1) + 4 * p4) * 2;
        const u32x2 a0 = lds_tr(a), a1 = lds_tr(a + 4 * 448);
        const u32x4 af = {a0.x, a0.y, a1.x, a1.y};
        acc[i][0] = mfma32(af, bt[0], acc[i][0]);
        acc[i][1] = mfma32(af, bt[1], acc[i][1]);
      }
    }
  };
  WG_BAR();
  wait_seg(0);
  issue(0);
  WG_BAR();
  finish(0);
  WAIT_VM0();
  WG_BAR();
  for (int s = 0; s < NSEG; ++s) {
    const bool more = s + 1 < NSEG;
    const bool early = more && (unsigned)(s + 1) < known;
    if (early) issue(s + 1);
    compute(s);
    if (more) {
      if (!early) { wait_seg(s + 1); issue(s + 1); }
      WG_BAR();
      finish(s + 1);
      WAIT_VM0();
      WG_BAR();
    }
  }
  if (wave == 0) wait_ge(rW, WS_PROG + (R_T5 + (lane < 18 ? lane : 0)) * 128, lane < 18, (unsigned)NSEG);
  WG_BAR();
#pragma unroll
  for (int j = 0; j < 2; ++j) {
    const unsigned trow = wt * 64 + j * 32 + (lane & 31);
    const float bb = bf2f(p.bsp[t0 + trow]);
#pragma unroll
    for (int i = 0; i < 3; ++i)
#pragma unroll
      for (int g = 0; g < 4; ++g) {
        float v[4];
#pragma unroll
        for (int r = 0; r < 4; ++r) v[r] = rbf(acc[i][j][4 * g + r]) + bb;
        lds_st(lds + trow * 400 + (wf * 96 + i * 32 + 8 * g + 4 * hh) * 2, u32x2{pk2(v[0], v[1]), pk2(v[2], v[3])});
      }
  }
  WG_BAR();
  {
    u16* y = (u16*)(p.ws + WS_Y);
#pragma unroll 4
    for (int j = 0; j < 12; ++j) {
      const unsigned id = tid + 512 * j, row = id / 24, ch = id % 24;
      const u32x4 zz = ld16c(rW, row * 9216u + f0 * 2u + ch * 16u, UNI(WS_Z + (unsigned)t0 * 9216u));
      const u32x4 gg = lds_ld<u32x4>(lds + row * 400 + ch * 16);
      const u32x4 o = {pk2(blo(zz.x) * blo(gg.x), bhi(zz.x) * bhi(gg.x)), pk2(blo(zz.y) * blo(gg.y), bhi(zz.y) * bhi(gg.y)),
                       pk2(blo(zz.z) * blo(gg.z), bhi(zz.z) * bhi(gg.z)), pk2(blo(zz.w) * blo(gg.w), bhi(zz.w) * bhi(gg.w))};
      *(GAS u32x4*)(y + (size_t)(t0 + row) * FH + f0 + ch * 8) = o;
    }
  }
}

__global__ void __launch_bounds__(256) k_out(const u16* __restrict__ y, const u16* __restrict__ wout, const u16* __restrict__ x1, const float* __restrict__ mod, u16* __restrict__ out) {
  extern __shared__ __attribute__((aligned(16))) unsigned char smem[];
  lptr lds = (lptr)smem;
  const int tid = threadIdx.x, lane = tid & 63, wave = __builtin_amdgcn_readfirstlane(tid >> 6);
  const int bid = blockIdx.x, xg = bid & 7, r = bid >> 3;
  const int tt = xg * 4 + r / 6, nt = r % 6, t0 = tt * 64, n0 = nt * 96;
  const int wt = wave & 1, wn = wave >> 1;
  const u16* ysrc[2]; const u16* wsrc[3];
#pragma unroll
  for (int j = 0; j < 2; ++j) { const unsigned row = (wave * 2 + j) * 8 + (lane >> 3), ch = (lane & 7) ^ ((row >> 1) & 7); ysrc[j] = y + (size_t)(t0 + row) * FH + ch * 8; }
#pragma unroll
  for (int j = 0; j < 3; ++j) { const unsigned L = (wave * 3 + j) * 1024 + lane * 16, row = L / 192u, ch = (L % 192u) >> 4; wsrc[j] = wout + (size_t)row * 576 + n0 + ch * 8; }
  auto stage = [&](int kt, int buf) {
#pragma unroll
    for (int j = 0; j < 2; ++j) glds16(ysrc[j] + kt * 64, UNI(buf * 20480 + (wave * 2 + j) * 1024));
#pragma unroll
    for (int j = 0; j < 3; ++j) glds16(wsrc[j] + (size_t)kt * 64 * 576, UNI(buf * 20480 + 8192 + (wave * 3 + j) * 1024));
  };
  f32x4 acc[2][3];
#pragma unroll
  for (int i = 0; i < 2; ++i)
#pragma unroll
    for (int j = 0; j < 3; ++j) acc[i][j] = f32x4{0, 0, 0, 0};
  const int i16 = lane & 15, g = lane >> 4, q = i16 >> 2, pp = lane & 3;
  stage(0, 0); stage(1, 1); stage(2, 2);
  for (int kt = 0; kt < 36; ++kt) {
    if (kt < 34) asm volatile("s_waitcnt vmcnt(10)" ::: "memory"); else if (kt == 34) asm volatile("s_waitcnt vmcnt(5)" ::: "memory"); else asm volatile("s_waitcnt vmcnt(0)" ::: "memory");
    WG_BAR();
    lptr yb = lds + (kt % 3) * 20480, wb = yb + 8192;
#pragma unroll
    for (int ks = 0; ks < 2; ++ks) {
      u32x4 bt[2];
#pragma unroll
      for (int i = 0; i < 2; ++i) { const unsigned row = wt * 32 + i * 16 + i16; bt[i] = lds_ld<u32x4>(yb + row * 128 + (((4 * ks + g) ^ ((row >> 1) & 7)) << 4)); }
#pragma unroll
      for (int j = 0; j < 3; ++j) {
        lptr a = wb + (32 * ks + 8 * g + q) * 192 + (wn * 48 + j * 16 + 4 * pp) * 2;
        const u32x2 a0 = lds_tr(a), a1 = lds_tr(a + 4 * 192);
        const u32x4 af = {a0.x, a0.y, a1.x, a1.y};
        acc[0][j] = mfma16(af, bt[0], acc[0][j]);
        acc[1][j] = mfma16(af, bt[1], acc[1][j]);
      }
    }
    WG_BAR();
    if (kt + 3 < 36) stage(kt + 3, kt % 3);
  }
#pragma unroll
  for (int i = 0; i < 2; ++i)
#pragma unroll
    for (int j = 0; j < 3; ++j) {
      const int t = t0 + wt * 32 + i * 16 + i16, n = n0 + wn * 48 + j * 16 + 4 * g;
      const u32x2 xv = *(const GAS u32x2*)(x1 + (size_t)t * 576 + n);
      const f32x4 gb = *(const GAS f32x4*)(mod + 5 * 576 + n);
      const float o0 = blo(xv.x) + rbf(gb[0] * rbf(acc[i][j][0])), o1 = bhi(xv.x) + rbf(gb[1] * rbf(acc[i][j][1]));
      const float o2 = blo(xv.y) + rbf(gb[2] * rbf(acc[i][j][2])), o3 = bhi(xv.y) + rbf(gb[3] * rbf(acc[i][j][3]));
      *(GAS u32x2*)(out + (size_t)t * 576 + n) = u32x2{pk2(o0, o1), pk2(o2, o3)};
    }
}

__global__ void __launch_bounds__(512, 2) k_mega(Params p) {
  extern __shared__ __attribute__((aligned(16))) unsigned char smem[];
  lptr lds = (lptr)smem;
  const int tid = threadIdx.x;
  const rsrc_t rW = mk_rsrc(p.ws, p.wsbytes);
  if (tid == 0) {
    const unsigned x = (unsigned)__builtin_amdgcn_s_getreg(20 | (3 << 11)) & 15u;
    GAS unsigned* tick = (GAS unsigned*)(p.ws + WS_TICK);
    unsigned r = __hip_atomic_fetch_add(tick + x * 32, 1u, __ATOMIC_RELAXED, __HIP_MEMORY_SCOPE_AGENT);
    unsigned slot;
    if (x < 8u && r < 32u) slot = x * 32u + r;
    else {
      unsigned k = __hip_atomic_fetch_add((GAS unsigned*)(p.ws + WS_OVF), 1u, __ATOMIC_RELAXED, __HIP_MEMORY_SCOPE_AGENT);
      slot = 0;
      for (unsigned sp = 0; sp < SPIN_CAP * 8u; ++sp) {
        unsigned sum = 0;
        for (int j = 0; j < 16; ++j) sum += __hip_atomic_load(tick + j * 32, __ATOMIC_RELAXED, __HIP_MEMORY_SCOPE_AGENT);
        if (sum >= gridDim.x) break;
        __builtin_amdgcn_s_sleep(8);
      }
      for (unsigned j = 0; j < 8u; ++j) {
        const unsigned n = min(__hip_atomic_load(tick + j * 32, __ATOMIC_RELAXED, __HIP_MEMORY_SCOPE_AGENT), 32u), fr = 32u - n;
        if (k < fr) { slot = j * 32u + n + k; break; }
        k -= fr;
      }
    }
    const unsigned xs = slot >> 5, rs = slot & 31u;
    unsigned role;
    if (xs < 4u) role = rs < 16u ? xs * 16u + rs : 64u + xs * 16u + (rs - 16u);
    else role = 128u + (xs - 4u) * 32u + rs;
    lds_st(lds, role);
  }
  __syncthreads();
  const int role = __builtin_amdgcn_readfirstlane((int)lds_ld<unsigned>(lds));
  __syncthreads();
  const unsigned long long wsa = (unsigned long long)p.ws;
  const unsigned exoff = (unsigned)(((wsa + WS_EX + 2097151ull) & ~2097151ull) - wsa);

  if (role < R_T4) {
    if (p.rolemask & 1u) {
      if (tid < 256) chain_path<true>(p, rW, lds, role, exoff); else chain_path<false>(p, rW, lds, role, exoff);
    }
  } else if (role < R_T5) {
    if (p.rolemask & 4u) t4_role(p, rW, lds, role - R_T4);
  } else if (role < R_LN) {
    if (p.rolemask & 16u) t5_role(p, rW, lds, role - R_T5);
  } else if (role < R_GQ) {
    if (p.rolemask & 8u) ln_role(p, rW, lds, role - R_LN);
  } else {
    const int gi = role - R_GQ;
    if (p.rolemask & 2u) {
      for (int r = 0; r * N_GQ + gi < 1536; ++r) {
        const int T = r * N_GQ + gi;
        qkv_tile(p, rW, lds, T / 96, T % 96);
        WAIT_VM0();
        WG_BAR();
        if (tid == 0) st4c(rW, WS_PROG + role * 128, 0, (unsigned)r + 1u);
        if (tid == 0) STAMP_AT(role, r);
      }
    }
    if ((p.rolemask & 32u) && gi < N_G) g_role(p, rW, lds, gi);
  }
  STAMP_AT(role, 250);
}

extern "C" void kernel_launch_dbg(void* const* d_in, void* d_out, void* d_ws, size_t ws_size, hipStream_t stream, unsigned stages, unsigned rolemask, unsigned t0, unsigned t1, unsigned t2) {
  Params p;
  p.x = (const u16*)d_in[0]; p.mem0 = (const u16*)d_in[1]; p.wq = (const u16*)d_in[2]; p.wk = (const u16*)d_in[3]; p.wv = (const u16*)d_in[4]; p.wo = (const u16*)d_in[5];
  p.win = (const u16*)d_in[8]; p.wsp = (const u16*)d_in[9]; p.bsp = (const u16*)d_in[10]; p.wout = (const u16*)d_in[11];
  p.ws = (unsigned char*)d_ws; p.out = (u16*)d_out; p.wsbytes = ws_size; p.rolemask = rolemask; p.tune0 = t0; p.tune1 = t1; p.tune2 = t2;
  if (stages & 1u) (void)hipMemsetAsync(d_ws, 0, WS_CTRL_BYTES, stream);
  if (stages & 16u) (void)hipMemsetAsync((char*)d_ws + WS_TICK, 0, WS_LNP - WS_TICK, stream);
  if (stages & 2u) {
    k_mod<<<108, 256, 0, stream>>>((const u16*)d_in[12], (const u16*)d_in[13], (float*)((char*)d_ws + WS_MOD));
    k_ln1<<<256, 512, 0, stream>>>(p.x, (const float*)((char*)d_ws + WS_MOD), (u16*)((char*)d_ws + WS_H));
  }
  if (stages & 4u) {
    (void)hipFuncSetAttribute((const void*)k_mega, hipFuncAttributeMaxDynamicSharedMemorySize, 163840);
    k_mega<<<256, 512, 163840, stream>>>(p);
  }
  if (stages & 8u) {
    (void)hipFuncSetAttribute((const void*)k_out, hipFuncAttributeMaxDynamicSharedMemorySize, 61440);
    k_out<<<192, 256, 61440, stream>>>((const u16*)((char*)d_ws + WS_Y), p.wout, (const u16*)((char*)d_ws + WS_X1), (const float*)((char*)d_ws + WS_MOD), p.out);
  }
}
extern "C" void kernel_launch(void* const* d_in, const int* in_sizes, int n_in, void* d_out, int out_size, void* d_ws, size_t ws_size, hipStream_t stream) {
  kernel_launch_dbg(d_in, d_out, d_ws, ws_size, stream, 15u, 0xffu, 9u, 5u, 0u);
}
```

```cpp
#include <hip/hip_runtime.h>
#include <stdint.h>

#define GAS __attribute__((address_space(1)))
#define LAS __attribute__((address_space(3)))
typedef unsigned short u16;
typedef __bf16 bf16x2 __attribute__((ext_vector_type(2)));
typedef __bf16 bf16x8 __attribute__((ext_vector_type(8)));
typedef __bf16 bf16x16 __attribute__((ext_vector_type(16)));
typedef short s16x4 __attribute__((ext_vector_type(4)));
typedef float f32x2 __attribute__((ext_vector_type(2)));
typedef float f32x4 __attribute__((ext_vector_type(4)));
typedef float f32x16 __attribute__((ext_vector_type(16)));
typedef unsigned u32x2 __attribute__((ext_vector_type(2)));
typedef unsigned u32x4 __attribute__((ext_vector_type(4)));
typedef unsigned u32x8 __attribute__((ext_vector_type(8)));
typedef LAS unsigned char* lptr;
typedef __amdgpu_buffer_rsrc_t rsrc_t;

constexpr int S_ = 2048, D_ = 576, HD_ = 4096, NSEG = 64, F_ = 4608, FH = 2304;
constexpr unsigned WS_PROG = 0;
constexpr unsigned WS_TICK = 32768;
constexpr unsigned WS_OVF = WS_TICK + 16 * 128;
constexpr unsigned WS_PAR = WS_TICK + 17 * 128;
constexpr unsigned WS_ABORT = WS_TICK + 18 * 128;
constexpr unsigned WS_CBAR = WS_TICK + 19 * 128;
constexpr unsigned WS_INI = 40960;
constexpr unsigned WS_LNP = 36864;
constexpr unsigned WS_MODP = WS_INI + 80 * 128;
constexpr unsigned WS_HP = WS_MODP + 54 * 128;
constexpr unsigned WS_YP = WS_HP + 80 * 128;
constexpr unsigned WS_CTRL_BYTES = WS_YP + 96 * 128;
constexpr unsigned WS_MOD = 98304;
constexpr unsigned WS_H = 131072;
constexpr unsigned WS_X1 = WS_H + S_ * D_ * 2;
constexpr unsigned WS_H2 = WS_X1 + S_ * D_ * 2;
constexpr unsigned WS_QKV = WS_H2 + S_ * D_ * 2;
constexpr unsigned WS_OX = WS_QKV + S_ * 12288 * 2;
constexpr unsigned WS_PT = WS_OX + S_ * HD_ * 2;
constexpr unsigned WS_Z = WS_PT + NSEG * 4 * 32 * D_ * 4;
constexpr unsigned WS_ZS = WS_Z + S_ * F_ * 2;
constexpr unsigned WS_Y = WS_ZS + NSEG * 18 * 32 * 8;
constexpr unsigned WS_EX = WS_Y + S_ * FH * 2;
constexpr unsigned EX_STEP = 2097152, EX_BYTES = 63 * EX_STEP;
constexpr unsigned WS_END = WS_EX + 2097152 + EX_BYTES;
constexpr unsigned WS_DBG = WS_END;
constexpr int R_CHAIN = 0, R_T4 = 64, R_T5 = 100, R_LN = 136, R_GQ = 144, N_GQ = 112, N_G = 96;
constexpr unsigned EMPTY = 0xFFFFFFFFu;
constexpr unsigned SPIN_CAP = 1u << 17;

struct Params {
  const u16* x; const u16* mem0; const u16* wq; const u16* wk; const u16* wv; const u16* wo;
  const u16* win; const u16* wsp; const u16* bsp; const u16* wout; const u16* c; const u16* wmod;
  unsigned char* ws; u16* out;
  unsigned long long wsbytes;
  unsigned rolemask; unsigned tune0; unsigned tune1; unsigned tune2;
};

#define UNI(x) ((unsigned)__builtin_amdgcn_readfirstlane((int)(x)))
#define UNI64(p) ((const unsigned char*)(((unsigned long long)UNI((unsigned long long)(p) >> 32) << 32) | (unsigned long long)UNI((unsigned)(unsigned long long)(p))))
#define MEMBAR() asm volatile("" ::: "memory")
#define WAIT_VM0() asm volatile("s_waitcnt vmcnt(0)" ::: "memory")
#define RAW_BAR() do { asm volatile("" ::: "memory"); __builtin_amdgcn_s_barrier(); asm volatile("" ::: "memory"); } while (0)
#define WG_BAR() do { asm volatile("s_waitcnt lgkmcnt(0)" ::: "memory"); __builtin_amdgcn_s_barrier(); asm volatile("" ::: "memory"); } while (0)
__device__ __forceinline__ unsigned pk2(float a, float b) { bf16x2 v = {(__bf16)a, (__bf16)b}; return __builtin_bit_cast(unsigned, v); }
__device__ __forceinline__ float rbf(float a) { return (float)(__bf16)a; }
__device__ __forceinline__ float blo(unsigned u) { return __builtin_bit_cast(float, u << 16); }
__device__ __forceinline__ float bhi(unsigned u) { return __builtin_bit_cast(float, u & 0xffff0000u); }
__device__ __forceinline__ float bf2f(u16 u) { return __builtin_bit_cast(float, (unsigned)u << 16); }
__device__ __forceinline__ rsrc_t mk_rsrc(const void* p, unsigned long long bytes) {
  const unsigned long long a = (unsigned long long)p;
  const unsigned lo = __builtin_amdgcn_readfirstlane((unsigned)a), hi = __builtin_amdgcn_readfirstlane((unsigned)(a >> 32));
  const unsigned n = __builtin_amdgcn_readfirstlane((unsigned)(bytes > 0xFFFFFFF0ull ? 0xFFFFFFF0ull : bytes));
  return __builtin_amdgcn_make_buffer_rsrc((void*)(((unsigned long long)hi << 32) | lo), 0, n, 0x00020000);
}
constexpr int SC1 = 16;
__device__ __forceinline__ u32x4 ld16c(rsrc_t r, unsigned vo, unsigned so = 0) { return __builtin_amdgcn_raw_buffer_load_b128(r, vo, so, SC1); }
__device__ __forceinline__ u32x4 ld16p(rsrc_t r, unsigned vo, unsigned so = 0) { return __builtin_amdgcn_raw_buffer_load_b128(r, vo, so, 0); }
__device__ __forceinline__ u32x2 ld8c(rsrc_t r, unsigned vo, unsigned so = 0) { return __builtin_amdgcn_raw_buffer_load_b64(r, vo, so, SC1); }
__device__ __forceinline__ unsigned ld4c(rsrc_t r, unsigned vo, unsigned so = 0) { return __builtin_amdgcn_raw_buffer_load_b32(r, vo, so, SC1); }
__device__ __forceinline__ void st16c(rsrc_t r, unsigned vo, unsigned so, u32x4 v) { __builtin_amdgcn_raw_buffer_store_b128(v, r, vo, so, SC1); asm volatile("s_nop 1" :: "v"(v)); }
__device__ __forceinline__ void st16p(rsrc_t r, unsigned vo, unsigned so, u32x4 v) { __builtin_amdgcn_raw_buffer_store_b128(v, r, vo, so, 0); asm volatile("s_nop 1" :: "v"(v)); }
__device__ __forceinline__ void st8c(rsrc_t r, unsigned vo, unsigned so, u32x2 v) { __builtin_amdgcn_raw_buffer_store_b64(v, r, vo, so, SC1); }
__device__ __forceinline__ void st4c(rsrc_t r, unsigned vo, unsigned so, unsigned v) { __builtin_amdgcn_raw_buffer_store_b32(v, r, vo, so, SC1); }
template <class T> __device__ __forceinline__ T lds_ld(lptr p) { return *(LAS T*)p; }
template <class T> __device__ __forceinline__ void lds_st(lptr p, T v) { *(LAS T*)p = v; }
typedef short v4i16_t __attribute__((ext_vector_type(4)));
__device__ __forceinline__ u32x2 lds_tr(lptr p) { return __builtin_bit_cast(u32x2, __builtin_amdgcn_ds_read_tr16_b64_v4i16((LAS v4i16_t*)p)); }
__device__ __forceinline__ void glds16(const void* gsrc, unsigned lds_dst) {
  unsigned keep;
  asm volatile("s_mov_b32 %0, m0\n\ts_mov_b32 m0, %2\n\ts_nop 0\n\tglobal_load_lds_dwordx4 %1, off\n\ts_mov_b32 m0, %0" : "=&s"(keep) : "v"(gsrc), "s"(lds_dst) : "memory");
}
__device__ __forceinline__ void glds16f(const void* sbase, unsigned voff, unsigned lds_dst) {
  asm volatile("s_mov_b32 m0, %2\n\ts_nop 0\n\tglobal_load_lds_dwordx4 %0, %1 sc1" :: "v"(voff), "s"(sbase), "s"(lds_dst) : "memory", "m0");
}
__device__ __forceinline__ void blds16p(u32x4 rs, unsigned voff, unsigned soff, unsigned lds_dst) {
  asm volatile("s_mov_b32 m0, %3\n\ts_nop 0\n\tbuffer_load_dwordx4 %0, %1, %2 offen lds" :: "v"(voff), "s"(rs), "s"(soff), "s"(lds_dst) : "memory", "m0");
}
__device__ __forceinline__ void blds16c(u32x4 rs, unsigned voff, unsigned soff, unsigned lds_dst) {
  asm volatile("s_mov_b32 m0, %3\n\ts_nop 0\n\tbuffer_load_dwordx4 %0, %1, %2 offen sc1 lds" :: "v"(voff), "s"(rs), "s"(soff), "s"(lds_dst) : "memory", "m0");
}
__device__ __forceinline__ u32x4 mk_rs4(const void* p, unsigned bytes) { return u32x4{UNI((unsigned)(unsigned long long)p), UNI((unsigned)((unsigned long long)p >> 32)), bytes, 0x00020000u}; }
__device__ __forceinline__ void glds16p(const void* sbase, unsigned voff, unsigned lds_dst) {
  asm volatile("s_mov_b32 m0, %2\n\ts_nop 0\n\tglobal_load_lds_dwordx4 %0, %1" :: "v"(voff), "s"(sbase), "s"(lds_dst) : "memory", "m0");
}
__device__ __forceinline__ void glds16c(const void* sbase, unsigned voff, unsigned lds_dst) {
  unsigned keep;
  asm volatile("s_mov_b32 %0, m0\n\ts_mov_b32 m0, %3\n\ts_nop 0\n\tglobal_load_lds_dwordx4 %1, %2 sc1\n\ts_mov_b32 m0, %0" : "=&s"(keep) : "v"(voff), "s"(sbase), "s"(lds_dst) : "memory");
}
__device__ __forceinline__ f32x4 mfma16(u32x4 a, u32x4 b, f32x4 c) { return __builtin_amdgcn_mfma_f32_16x16x32_bf16(__builtin_bit_cast(bf16x8, a), __builtin_bit_cast(bf16x8, b), c, 0, 0, 0); }
__device__ __forceinline__ f32x4 mfma16k16(u32x2 a, u32x2 b, f32x4 c) { return __builtin_amdgcn_mfma_f32_16x16x16bf16_1k(__builtin_bit_cast(s16x4, a), __builtin_bit_cast(s16x4, b), c, 0, 0, 0); }
__device__ __forceinline__ f32x16 mfma32(u32x4 a, u32x4 b, f32x16 c) { return __builtin_amdgcn_mfma_f32_32x32x16_bf16(__builtin_bit_cast(bf16x8, a), __builtin_bit_cast(bf16x8, b), c, 0, 0, 0); }
__device__ __forceinline__ f32x4 smfmac(u32x4 a, u32x8 b, f32x4 c, int idx) { return __builtin_amdgcn_smfmac_f32_16x16x64_bf16(__builtin_bit_cast(bf16x8, a), __builtin_bit_cast(bf16x16, b), c, idx, 0, 0); }
__device__ __forceinline__ float xr16(float a, bool mx) {
  const unsigned u = __builtin_bit_cast(unsigned, a);
  const auto r = __builtin_amdgcn_permlane16_swap(u, u, false, false);
  const float p = __builtin_bit_cast(float, (unsigned)r[0]), q = __builtin_bit_cast(float, (unsigned)r[1]);
  return mx ? fmaxf(p, q) : p + q;
}
__device__ __forceinline__ float xr32(float a, bool mx) {
  const unsigned u = __builtin_bit_cast(unsigned, a);
  const auto r = __builtin_amdgcn_permlane32_swap(u, u, false, false);
  const float p = __builtin_bit_cast(float, (unsigned)r[0]), q = __builtin_bit_cast(float, (unsigned)r[1]);
  return mx ? fmaxf(p, q) : p + q;
}
template <int CTRL> __device__ __forceinline__ float dppf(float a) { return __builtin_bit_cast(float, __builtin_amdgcn_update_dpp(0, __builtin_bit_cast(int, a), CTRL, 0xf, 0xf, true)); }
__device__ __forceinline__ float wave_sum(float v) {
  v += dppf<0xB1>(v);
  v += dppf<0x4E>(v);
  v += dppf<0x141>(v);
  v += dppf<0x140>(v);
  v = xr16(v, false); v = xr32(v, false);
  return v;
}
#define LST(i) do {} while (0)
#define TS(i) do {} while (0)
#define STAMP_CLK(i) do {} while (0)
#define KST(wsp, k, e) do {} while (0)
__device__ __forceinline__ unsigned long long clk() { return __builtin_amdgcn_s_memtime(); }
#define ACC(k, a, b) do {} while (0)
#define ACLK(x) do {} while (0)
#define STAMP_AT(role, i) do {} while (0)
#define STAMP_CLK_OFF(i) do {} while (0)

template <int SLEEP = 1> __device__ __forceinline__ unsigned wait_ge(rsrc_t rW, unsigned off, bool act, unsigned target) {
  for (unsigned sp = 0;; ++sp) {
    MEMBAR();
    const unsigned v = ld4c(rW, act ? off : WS_ABORT);
    const bool ok = act ? (v >= target) : true;
    if (__all(ok)) return v;
    if ((sp & 63u) == 63u) {
      if (__builtin_amdgcn_readfirstlane(ld4c(rW, WS_ABORT)) != 0u) return v;
      if (sp > SPIN_CAP) { st4c(rW, WS_ABORT, 0, 1u); return v; }
    }
    __builtin_amdgcn_s_sleep(SLEEP);
  }
}

__global__ void __launch_bounds__(256) k_mod(const u16* __restrict__ c, const u16* __restrict__ wmod, float* __restrict__ mod) {
  __shared__ float sc[576];
  __shared__ float red[4][32];
  KST((unsigned char*)mod - WS_MOD, 0, 0);
  const int tid = threadIdx.x, b = blockIdx.x, lane = tid & 63, wave = tid >> 6;
  for (int i = tid; i < 576; i += 256) { const float v = bf2f(c[i]); sc[i] = rbf(v / (1.0f + __expf(-v))); }
  __syncthreads();
  const int kg = tid >> 2, cg = tid & 3;
  float acc[8];
#pragma unroll
  for (int j = 0; j < 8; ++j) acc[j] = 0.f;
  u32x4 w[9];
#pragma unroll
  for (int i = 0; i < 9; ++i) w[i] = *(const GAS u32x4*)(wmod + (size_t)(kg + 64 * i) * 3456 + 32 * b + 8 * cg);
#pragma unroll
  for (int i = 0; i < 9; ++i) {
    const float s = sc[kg + 64 * i];
    acc[0] += s * blo(w[i].x); acc[1] += s * bhi(w[i].x); acc[2] += s * blo(w[i].y); acc[3] += s * bhi(w[i].y);
    acc[4] += s * blo(w[i].z); acc[5] += s * bhi(w[i].z); acc[6] += s * blo(w[i].w); acc[7] += s * bhi(w[i].w);
  }
#pragma unroll
  for (int j = 0; j < 8; ++j) {
    float v = acc[j];
    v += __shfl_xor(v, 4); v += __shfl_xor(v, 8); v += __shfl_xor(v, 16); v += __shfl_xor(v, 32);
    if (lane < 4) red[wave][8 * cg + j] = v;
  }
  __syncthreads();
  if (tid < 32) mod[32 * b + tid] = rbf(red[0][tid] + red[1][tid] + red[2][tid] + red[3][tid]);
  KST((unsigned char*)mod - WS_MOD, 0, 1);
}

__device__ __forceinline__ void adaln_row(float (&v)[16], int lane, const float* __restrict__ cc, const float* __restrict__ ss, u32x4& o0, u32x4& o1) {
  const bool two = lane < 8;
  float s = 0.f;
#pragma unroll
  for (int j = 0; j < 8; ++j) s += v[j] + (two ? v[8 + j] : 0.f);
  const float mean = wave_sum(s) * (1.0f / 576.0f);
  float q = 0.f;
#pragma unroll
  for (int j = 0; j < 8; ++j) { const float d = v[j] - mean; q += d * d; const float e = v[8 + j] - mean; q += two ? e * e : 0.f; }
  const float var = wave_sum(q) * (1.0f / 576.0f);
  const float mb = rbf(mean), sd = rbf(__builtin_sqrtf(rbf(rbf(var) + 1e-5f)));
  float r[16];
#pragma unroll
  for (int j = 0; j < 16; ++j) {
    const int col = (j < 8 ? 8 * lane : 512 + 8 * (lane & 7)) + (j & 7);
    const float n = rbf(rbf(v[j] - mb) / sd);
    r[j] = rbf(n * rbf(1.0f + cc[col])) + ss[col];
  }
  o0 = u32x4{pk2(r[0], r[1]), pk2(r[2], r[3]), pk2(r[4], r[5]), pk2(r[6], r[7])};
  o1 = u32x4{pk2(r[8], r[9]), pk2(r[10], r[11]), pk2(r[12], r[13]), pk2(r[14], r[15])};
}
__device__ __forceinline__ void adaln_row_r(float (&v)[16], int lane, const float (&c1)[16], const float (&ss)[16], u32x4& o0, u32x4& o1) {
  const bool two = lane < 8;
  float s = 0.f;
#pragma unroll
  for (int j = 0; j < 8; ++j) s += v[j] + (two ? v[8 + j] : 0.f);
  const float mean = wave_sum(s) * (1.0f / 576.0f);
  float q = 0.f;
#pragma unroll
  for (int j = 0; j < 8; ++j) { const float d = v[j] - mean; q += d * d; const float e = v[8 + j] - mean; q += two ? e * e : 0.f; }
  const float var = wave_sum(q) * (1.0f / 576.0f);
  const float mb = rbf(mean), rs = 1.0f / rbf(__builtin_sqrtf(rbf(rbf(var) + 1e-5f)));
  float r[16];
#pragma unroll
  for (int j = 0; j < 16; ++j) r[j] = rbf(rbf(rbf(v[j] - mb) * rs) * c1[j]) + ss[j];
  o0 = u32x4{pk2(r[0], r[1]), pk2(r[2], r[3]), pk2(r[4], r[5]), pk2(r[6], r[7])};
  o1 = u32x4{pk2(r[8], r[9]), pk2(r[10], r[11]), pk2(r[12], r[13]), pk2(r[14], r[15])};
}
__device__ __forceinline__ void unpack8(u32x4 w, float* v) {
  v[0] = blo(w.x); v[1] = bhi(w.x); v[2] = blo(w.y); v[3] = bhi(w.y); v[4] = blo(w.z); v[5] = bhi(w.z); v[6] = blo(w.w); v[7] = bhi(w.w);
}
__global__ void __launch_bounds__(512) k_ln1(const u16* __restrict__ x, const float* __restrict__ mod, u16* __restrict__ h) {
  KST((unsigned char*)h - WS_H, 1, 0);
  const int lane = threadIdx.x & 63, row = blockIdx.x * 8 + (threadIdx.x >> 6);
  float v[16];
  const u32x4 a = *(const GAS u32x4*)(x + (size_t)row * 576 + 8 * lane);
  const u32x4 b = *(const GAS u32x4*)(x + (size_t)row * 576 + 512 + 8 * (lane & 7));
  unpack8(a, v); unpack8(b, v + 8);
  u32x4 o0, o1;
  adaln_row(v, lane, mod + 576, mod, o0, o1);
  *(GAS u32x4*)(h + (size_t)row * 576 + 8 * lane) = o0;
  if (lane < 8) *(GAS u32x4*)(h + (size_t)row * 576 + 512 + 8 * lane) = o1;
  KST((unsigned char*)h - WS_H, 1, 1);
}

constexpr unsigned L_WO = 0, L_AIM = 73728, L_QIM = 82944, L_KIM = 85120, L_VIM = 87296, L_VPX = 88320, L_OIM = 90368, L_TILE = 91520,
                   TILE_B = 20480, T_K = 0, T_Q = 8192, T_V = 16384, L_OXS = 132480, L_CNT = 137088, L_STAMP = 137216, L_WV = 137728, L_STG = 73728;
__device__ __forceinline__ int opaque(int v) { asm volatile("" : "+v"(v)); return v; }
#define SCHED() __builtin_amdgcn_sched_barrier(0)
constexpr unsigned P_UNIT = 16384;
constexpr float SCALE = 0.08838834764831845f, LOG2E = 1.4426950408889634f;
__device__ __forceinline__ unsigned ex_span(unsigned exoff, unsigned pw, int s) { return exoff + (unsigned)s * EX_STEP + (((pw >> (s & 1)) & 1u) << 13); }
__device__ __forceinline__ void ex_measure(rsrc_t rW, unsigned exoff, int i) {
  if (threadIdx.x < 64) {
    const int lane = threadIdx.x;
    const unsigned a = UNI(exoff + (i >> 1) * 2097152u + 100u * 16384u + (i & 1) * 8192u);
    unsigned best = 0xffffffffu;
    for (int r = 0; r < 24; ++r) {
      const unsigned long long t0 = clk();
      st4c(rW, lane * 4 + (r & 7) * 256u, a, EMPTY);
      WAIT_VM0();
      best = min(best, (unsigned)(clk() - t0));
      __builtin_amdgcn_s_sleep(4);
    }
    st4c(rW, lane == 0 ? WS_PAR + i * 4 : 0x80000000u, 0, best | 1u);
  }
  __syncthreads();
}
__device__ __forceinline__ unsigned ex_parity(rsrc_t rW, int lane) {
  wait_ge(rW, WS_PAR + (lane & 3) * 4, true, 1u);
  const u32x4 b = ld16c(rW, WS_PAR);
  return UNI((b.y > b.x ? 1u : 0u) | (b.w > b.z ? 2u : 0u));
}
__device__ __forceinline__ void ex_init(rsrc_t rW, unsigned exoff, unsigned pw, int i80, int s0, int s1) {
  const int tid = threadIdx.x, lane = tid & 63, wave = __builtin_amdgcn_readfirstlane(tid >> 6);
  const unsigned em = (unsigned)opaque(-1), k = i80 * 8 + wave;
  const u32x4 e4 = {em, em, em, em};
  if (s1 > NSEG - 1) s1 = NSEG - 1;
  if (s0 >= s1) return;
  for (int s = s0; s < s1; ++s)
    if (k < 592) {
      st16c(rW, lane * 16 + (k & 7) * 1024, UNI(ex_span(exoff, pw & 3u, s) + (k >> 3) * 16384), e4);
      if (pw > 3u) st16c(rW, lane * 16 + (k & 7) * 1024, UNI(ex_span(exoff, 3u - (pw & 3u), s) + (k >> 3) * 16384), e4);
    }
  WAIT_VM0();
  WG_BAR();
  if (tid == 0) st4c(rW, WS_INI + i80 * 128, 0, (unsigned)s1);
}
__device__ __forceinline__ unsigned umax4(u32x4 v) { return max(max(v.x, v.y), max(v.z, v.w)); }
__device__ __forceinline__ float fmax2(float a, float b) { return __builtin_elementwise_maximum(a, b); }
__device__ __forceinline__ float xmax16(float a) { const unsigned u = __builtin_bit_cast(unsigned, a); const auto r = __builtin_amdgcn_permlane16_swap(u, u, false, false);
  return fmax2(__builtin_bit_cast(float, (unsigned)r[0]), __builtin_bit_cast(float, (unsigned)r[1])); }
__device__ __forceinline__ float xmax32(float a) { const unsigned u = __builtin_bit_cast(unsigned, a); const auto r = __builtin_amdgcn_permlane32_swap(u, u, false, false);
  return fmax2(__builtin_bit_cast(float, (unsigned)r[0]), __builtin_bit_cast(float, (unsigned)r[1])); }

__device__ __forceinline__ void poll8(u32x4 (&v)[8], unsigned bad, unsigned vo, u32x4 rs, unsigned sA, unsigned sB) {
  asm volatile(
      "s_bitcmp1_b32 %[bad], 0\n\ts_cbranch_scc0 Lp%=_0\n\tbuffer_load_dwordx4 %0, %[vo], %[rs], %[sA] offen sc1\nLp%=_0:\n\t"
      "s_bitcmp1_b32 %[bad], 1\n\ts_cbranch_scc0 Lp%=_1\n\tbuffer_load_dwordx4 %1, %[vo], %[rs], %[sA] offen offset:1024 sc1\nLp%=_1:\n\t"
      "s_bitcmp1_b32 %[bad], 2\n\ts_cbranch_scc0 Lp%=_2\n\tbuffer_load_dwordx4 %2, %[vo], %[rs], %[sA] offen offset:2048 sc1\nLp%=_2:\n\t"
      "s_bitcmp1_b32 %[bad], 3\n\ts_cbranch_scc0 Lp%=_3\n\tbuffer_load_dwordx4 %3, %[vo], %[rs], %[sA] offen offset:3072 sc1\nLp%=_3:\n\t"
      "s_bitcmp1_b32 %[bad], 4\n\ts_cbranch_scc0 Lp%=_4\n\tbuffer_load_dwordx4 %4, %[vo], %[rs], %[sB] offen sc1\nLp%=_4:\n\t"
      "s_bitcmp1_b32 %[bad], 5\n\ts_cbranch_scc0 Lp%=_5\n\tbuffer_load_dwordx4 %5, %[vo], %[rs], %[sB] offen offset:1024 sc1\nLp%=_5:\n\t"
      "s_bitcmp1_b32 %[bad], 6\n\ts_cbranch_scc0 Lp%=_6\n\tbuffer_load_dwordx4 %6, %[vo], %[rs], %[sB] offen offset:2048 sc1\nLp%=_6:\n\t"
      "s_bitcmp1_b32 %[bad], 7\n\ts_cbranch_scc0 Lp%=_7\n\tbuffer_load_dwordx4 %7, %[vo], %[rs], %[sB] offen offset:3072 sc1\nLp%=_7:\n\t"
      "s_waitcnt vmcnt(0)"
      : "+v"(v[0]), "+v"(v[1]), "+v"(v[2]), "+v"(v[3]), "+v"(v[4]), "+v"(v[5]), "+v"(v[6]), "+v"(v[7])
      : [bad] "s"(bad), [vo] "v"(vo), [rs] "s"(rs), [sA] "s"(sA), [sB] "s"(sB)
      : "memory", "scc");
}
__device__ __forceinline__ void poll5(u32x4 (&v)[5], unsigned bad, unsigned vo, u32x4 rs, unsigned s0, unsigned s2, unsigned s4) {
  asm volatile(
      "s_bitcmp1_b32 %[bad], 0\n\ts_cbranch_scc0 Lq%=_0\n\tbuffer_load_dwordx4 %0, %[vo], %[rs], %[s0] offen sc1\nLq%=_0:\n\t"
      "s_bitcmp1_b32 %[bad], 1\n\ts_cbranch_scc0 Lq%=_1\n\tbuffer_load_dwordx4 %1, %[vo], %[rs], %[s0] offen offset:2048 sc1\nLq%=_1:\n\t"
      "s_bitcmp1_b32 %[bad], 2\n\ts_cbranch_scc0 Lq%=_2\n\tbuffer_load_dwordx4 %2, %[vo], %[rs], %[s2] offen sc1\nLq%=_2:\n\t"
      "s_bitcmp1_b32 %[bad], 3\n\ts_cbranch_scc0 Lq%=_3\n\tbuffer_load_dwordx4 %3, %[vo], %[rs], %[s2] offen offset:2048 sc1\nLq%=_3:\n\t"
      "s_bitcmp1_b32 %[bad], 4\n\ts_cbranch_scc0 Lq%=_4\n\tbuffer_load_dwordx4 %4, %[vo], %[rs], %[s4] offen sc1\nLq%=_4:\n\t"
      "s_waitcnt vmcnt(0)"
      : "+v"(v[0]), "+v"(v[1]), "+v"(v[2]), "+v"(v[3]), "+v"(v[4])
      : [bad] "s"(bad), [vo] "v"(vo), [rs] "s"(rs), [s0] "s"(s0), [s2] "s"(s2), [s4] "s"(s4)
      : "memory", "scc");
}
template <int N> struct IC { static constexpr int value = N; };
__device__ __forceinline__ int chain_piece(int wave, int j) { return j < 2 ? 2 * wave + j : (j < 4 ? 16 + 2 * wave + (j - 2) : 32 + wave); }
template <int PITCH> __device__ __forceinline__ u32x8 chain_read_w(lptr img, int cw, int lane) {
  const int gb = lane >> 4, q = (lane & 15) >> 2, pp = lane & 3;
  u32x2 t[4];
#pragma unroll
  for (int a = 0; a < 4; ++a) {
    const int row = 32 * (a & 1) + 16 * (q & 1) + 8 * (q >> 1) + gb + 4 * (a >> 1);
    const int sw = PITCH == 256 ? (((row & 1) | (((row >> 3) & 3) << 1)) << 1) : (((row >> 3) & 3) << 1);
    t[a] = lds_tr(img + row * PITCH + (((2 * cw + (pp >> 1)) ^ sw) << 4) + 8 * (pp & 1));
  }
  return u32x8{t[0].x, t[0].y, t[1].x, t[1].y, t[2].x, t[2].y, t[3].x, t[3].y};
}
__device__ __forceinline__ u32x8 chain_wo_frag(lptr f) {
  const u32x4 a = lds_ld<u32x4>(f), b = lds_ld<u32x4>(f + 1024);
  return u32x8{a.x, a.y, a.z, a.w, b.x, b.y, b.z, b.w};
}
__device__ __forceinline__ unsigned chain_pk_tile(f32x4 d) { return min(pk2(d[0] + d[2], d[1] + d[3]), 0xFFFEFFFFu); }

__device__ __forceinline__ void chain_wait_tiles(rsrc_t rW, int h, int seg, int lane_) {
  const int lane = opaque(lane_);
  wait_ge(rW, WS_PROG + (R_GQ + (lane < 3 ? lane : 0) * 32 + h) * 128, lane < 3, seg + 1);
}
__device__ __forceinline__ void chain_tile_pieces(rsrc_t rW, lptr lds, unsigned segoff, unsigned dst, int z, int w4, int lane_) {
  const int lane = opaque(lane_);
  const unsigned t1 = (lane >> 4) * 24576u + (lane & 15) * 16, t0 = (lane & 15) ^ (lane >> 4);
  const unsigned kq = UNI(segoff + (w4 & 1) * 393216 + (w4 < 2 ? 8192 : 0));
  u32x4 v[5];
#pragma unroll
  for (int i = 0; i < 4; ++i) v[i] = ld16c(rW, t1, kq + i * 98304);
  v[4] = ld16c(rW, (lane >> 3) * 24576u + (lane & 7) * 16, UNI(segoff + w4 * 196608 + 16384 + z * 128));
  lptr d = lds + dst + w4 * 4096 + (lane >> 4) * 256;
#pragma unroll
  for (int i = 0; i < 4; ++i) lds_st(d + i * 1024 + ((t0 ^ (4 * i)) << 4), v[i]);
  lds_st(lds + dst + (16 + w4) * 1024 + (lane >> 3) * 128 + (((lane & 7) ^ (((lane >> 4) & 3) << 1)) << 4), v[4]);
}

__device__ __forceinline__ void chain_tile_dma(const unsigned char* segb, unsigned dst, int z, int w4, int i, unsigned t0, unsigned t1, unsigned t2) {
  if (i < 4) glds16f(segb + (w4 & 1) * 393216 + (w4 < 2 ? 8192 : 0) + i * 98304, t1 + ((t0 ^ (4 * i)) << 4), UNI(dst + w4 * 4096 + i * 1024));
  else glds16f(segb + w4 * 196608 + 16384 + z * 128, t2, UNI(dst + (16 + w4) * 1024));
}
__device__ __forceinline__ void chain_token_attn(lptr lds, int seg, int qt, int lane_) {
  const int lane = opaque(lane_);
  lptr tb = lds + L_TILE + (seg & 1) * TILE_B;
  const int i = lane & 15, g = lane >> 4;
  f32x4 s0 = {0, 0, 0, 0}, s1 = s0, s2 = s0;
  {
    u32x4 bq[4], am[4], a1[4], a2[4];
    lptr kim = lds + L_KIM + (2 * (i >> 2) + (i & 1)) * 272 + g * 16;
#pragma unroll
    for (int e = 0; e < 4; ++e) {
      const unsigned o = i * 256 + (((4 * e + g) ^ i) << 4);
      bq[e] = lds_ld<u32x4>(tb + T_Q + qt * 4096 + o);
      am[e] = lds_ld<u32x4>(kim + e * 64);
      a1[e] = lds_ld<u32x4>(tb + T_K + o);
      if (qt) a2[e] = lds_ld<u32x4>(tb + T_K + 4096 + o);
    }
    SCHED();
#pragma unroll
    for (int e = 0; e < 4; ++e) { s0 = mfma16(am[e], bq[e], s0); s1 = mfma16(a1[e], bq[e], s1); if (qt) s2 = mfma16(a2[e], bq[e], s2); }
  }
  const int q = i >> 2, pp = lane & 3;
  u32x2 va[4][2]; unsigned vm[4];
#pragma unroll
  for (int t = 0; t < 4; ++t) {
    const unsigned vr = 4 * g + q, vo = vr * 128 + (((2 * t + (pp >> 1)) ^ (((vr >> 1) & 3) << 1)) << 4) + 8 * (pp & 1);
    va[t][0] = lds_tr(tb + T_V + vo); va[t][1] = lds_tr(tb + T_V + 16 * 128 + vo);
    vm[t] = lds_ld<unsigned>(lds + L_VIM + t * 256 + lane * 4);
  }
  SCHED();
  const int qi = 16 * qt + i;
  const float NEG = __builtin_bit_cast(float, opaque(0xCE6E6B28));
  float v[10];
  v[0] = rbf(rbf(s0[0]) * SCALE); v[1] = rbf(rbf(s0[1]) * SCALE);
#pragma unroll
  for (int r = 0; r < 4; ++r) {
    v[2 + r] = (4 * g + r <= qi) ? rbf(rbf(s1[r]) * SCALE) : NEG;
    v[6 + r] = (qt && 16 + 4 * g + r <= qi) ? rbf(rbf(s2[r]) * SCALE) : NEG;
  }
  float mx = fmax2(fmax2(fmax2(v[0], v[1]), fmax2(v[2], v[3])), fmax2(fmax2(fmax2(v[4], v[5]), fmax2(v[6], v[7])), fmax2(v[8], v[9])));
  mx = xmax32(xmax16(mx));
  float sum = 0.f;
#pragma unroll
  for (int r = 0; r < 10; ++r) { v[r] = __builtin_amdgcn_exp2f((v[r] - mx) * LOG2E); sum += v[r]; }
  sum = xr32(xr16(sum, false), false);
  const float inv = 1.0f / sum;
  const u32x4 pt = {pk2(v[2] * inv, v[3] * inv), pk2(v[4] * inv, v[5] * inv), pk2(v[6] * inv, v[7] * inv), pk2(v[8] * inv, v[9] * inv)};
  const u32x4 pm = {pk2(v[0] * inv, v[1] * inv), 0u, 0u, 0u};
  lptr ob = lds + L_OXS + qt * 2304;
#pragma unroll
  for (int t = 0; t < 4; ++t) {
    f32x4 o = mfma16(u32x4{va[t][0].x, va[t][0].y, va[t][1].x, va[t][1].y}, pt, f32x4{0, 0, 0, 0});
    o = mfma16(u32x4{vm[t], 0u, 0u, 0u}, pm, o);
    lds_st(ob + i * 144 + (16 * t + 4 * g) * 2, u32x2{pk2(o[0], o[1]), pk2(o[2], o[3])});
  }
}
__device__ __forceinline__ void chain_store_ox(rsrc_t rW, lptr lds, int h, int z, int seg, int qt, int lane_) {
  const int lane = opaque(lane_);
  lptr ob = lds + L_OXS + qt * 2304;
  const unsigned so = UNI(WS_OX + ((unsigned)seg * 32u + 16 * qt) * 8192u + h * 256u + z * 128u);
#pragma unroll
  for (int j = 0; j < 2; ++j) {
    const unsigned row = 8 * j + (lane >> 3);
    const u32x4 w = lds_ld<u32x4>(ob + row * 144 + (lane & 7) * 16);
    st16c(rW, row * 8192u + (lane & 7) * 16, so, w);
  }
}
__device__ __forceinline__ void chain_raise(const Params& p, int c, unsigned done, int lane_) {
  if (opaque(lane_) == 0) (void)__hip_atomic_fetch_max((GAS unsigned*)(p.ws + WS_PROG + (R_CHAIN + c) * 128), done, __ATOMIC_RELAXED, __HIP_MEMORY_SCOPE_AGENT);
}
__device__ __forceinline__ void chain_publish_ox(const Params& p, lptr lds, int c, unsigned done, int lane_) {
  const int lane = opaque(lane_);
  WAIT_VM0();
  unsigned old = 0;
  if (lane == 0) old = __hip_atomic_fetch_add((LAS unsigned*)(lds + opaque(L_CNT)), 1u, __ATOMIC_RELAXED, __HIP_MEMORY_SCOPE_WORKGROUP);
  old = __builtin_amdgcn_readfirstlane(old);
  if ((old & 1u) && lane == 0) (void)__hip_atomic_fetch_max((GAS unsigned*)(p.ws + WS_PROG + (R_CHAIN + c) * 128), done, __ATOMIC_RELAXED, __HIP_MEMORY_SCOPE_AGENT);
}

#define GATHER_DBG
#define GATHER_DBG_ARGS
struct Pause { unsigned d, ok; };
__device__ __forceinline__ void pause_adapt(Pause& a, unsigned base, bool first_ok, unsigned mode) {
  if (mode & 256u) return;
  if (!first_ok) { a.d = min(a.d + 2u, base + 10u); a.ok = 0; }
  else if (++a.ok >= 6u && a.d > base) { a.d -= 1u; a.ok = 0; }
}
__device__ __forceinline__ void chain_gather(const Params& p, rsrc_t rW, lptr lds, int s, int kb0, bool two, unsigned gnext, Pause& pg, int lane_ GATHER_DBG_ARGS) {
  const unsigned g0 = UNI(gnext + (kb0 >> 3) * P_UNIT + (kb0 & 7) * 1024), g1 = UNI(gnext + ((kb0 + 1) >> 3) * P_UNIT + ((kb0 + 1) & 7) * 1024);
  const int lane = opaque(lane_);
  const unsigned lo16 = lane * 16, lo2 = two ? lo16 : 0x80000000u;
  const unsigned gslot = ((lane & 3) * 16 + 4 * (lane >> 4) + 2 * ((lane >> 3) & 1) + ((lane >> 2) & 1)) * 16;
  for (unsigned sp = 0; sp < SPIN_CAP * 16u; ++sp)
    if (*(volatile LAS unsigned*)(lds + L_CNT + 8) > (unsigned)s) break;
  ACLK(tfl); ACC(1, tfl, tb2);
  LST(6);
  TS(11);
  for (unsigned i = 0; i < pg.d; ++i) __builtin_amdgcn_s_sleep(1);
  TS(12);
  u32x4 v0, v1;
  unsigned sp = 0;
  for (;; ++sp) {
    MEMBAR();
    v0 = ld16c(rW, lo16, g0); v1 = ld16c(rW, lo2, g1);
    const unsigned mx = max(umax4(v0), umax4(v1));
    if (__builtin_expect(__all(mx != EMPTY), 1)) break;
    if ((sp & 63u) == 63u) {
      if (__builtin_amdgcn_readfirstlane(ld4c(rW, WS_ABORT)) != 0u) break;
      if (sp > SPIN_CAP) { st4c(rW, WS_ABORT, 0, 3u); break; }
    }
  }
  pause_adapt(pg, p.tune1, sp == 0, p.tune2);
  ACLK(tgn); ACC(2, tgn, tb2);
  LST(7);
  TS(13);
  lptr ad = lds + L_AIM + kb0 * 1024 + gslot;
  lds_st(ad, v0);
  if (two) lds_st(ad + 1024, v1);
}

template <bool AW>
__device__ __forceinline__ void chain_path(const Params& p, rsrc_t rW, lptr lds, int c, unsigned exoff) {
  const int tid = threadIdx.x, wave = __builtin_amdgcn_readfirstlane(tid >> 6);
  int lane = tid & 63;
  const int h = c >> 1, z = c & 1;
  constexpr int NV = AW ? 2 : 4, V0 = AW ? 0 : 2;
  u32x8 Wq[9], Wk[9], Wv[NV];
  if (wave == 2) STAMP_AT(c, 0);
  {
    const u16* wo = p.wo + (size_t)(h * 128 + z * 64) * 576;
    const unsigned vqk = ((lane >> 4) * 8192u + (((lane & 15) ^ (((lane >> 4) & 1) << 1)) << 4)) ^ ((wave & 3) << 6), vv = ((lane >> 3) * 8192u + ((lane & 7) << 4)) ^ ((wave & 3) << 5);
    {
      const u32x4 dq = mk_rs4(p.wq, 576 * 4096 * 2), dk = mk_rs4(p.wk, 576 * 4096 * 2), dv = mk_rs4(p.wv, 576 * 4096 * 2);
#pragma unroll
      for (int kb = 0; kb < 4; ++kb) {
#pragma unroll
        for (int j = 0; j < 4; ++j) blds16p(j < 2 ? dq : dk, vqk, UNI((kb * 64 + (2 * wave + (j & 1)) * 4) * 8192 + h * 256), UNI(kb * 40960 + chain_piece(wave, j) * 1024));
        blds16p(dv, vv, UNI((kb * 64 + wave * 8) * 8192 + h * 256 + z * 128), UNI(kb * 40960 + chain_piece(wave, 4) * 1024));
      }
    }
    u32x4 rw[5][5], ro[9];
    const rsrc_t rq = mk_rsrc(p.wq, 576 * 4096 * 2), rk = mk_rsrc(p.wk, 576 * 4096 * 2), rv = mk_rsrc(p.wv, 576 * 4096 * 2), ro_ = mk_rsrc(wo, 64 * 576 * 2);
    auto raw = [&](int kb, int j) -> u32x4 {
      if (j < 4) return ld16p(j < 2 ? rq : rk, vqk, UNI((kb * 64 + (2 * wave + (j & 1)) * 4) * 8192 + h * 256));
      return ld16p(rv, vv, UNI((kb * 64 + wave * 8) * 8192 + h * 256 + z * 128));
    };
#pragma unroll
    for (int j = 0; j < 9; ++j) ro[j] = ld16p(ro_, lane * 16, UNI((wave * 9 + j) * 1024));
#pragma unroll
    for (int kb = 4; kb < 9; ++kb)
#pragma unroll
      for (int j = 0; j < 5; ++j) rw[kb - 4][j] = raw(kb, j);
    u32x8 tv[3];
    auto take = [&](auto KB, lptr img) {
      constexpr int kb = decltype(KB)::value;
      Wq[kb] = chain_read_w<256>(img, wave, lane);
      Wk[kb] = chain_read_w<256>(img + 16384, wave, lane);
      if constexpr (kb >= V0 && kb < V0 + NV) Wv[kb - V0] = chain_read_w<128>(img + 32768, wave & 3, lane);
      if constexpr (!AW && kb >= 6) tv[kb - 6] = chain_read_w<128>(img + 32768, wave & 3, lane);
    };
    asm volatile("s_waitcnt vmcnt(44)" ::: "memory");
    WG_BAR();
    take(IC<0>{}, lds); take(IC<1>{}, lds + 40960);
    asm volatile("s_waitcnt vmcnt(34)" ::: "memory");
    WG_BAR();
#pragma unroll
    for (int j = 0; j < 9; ++j) lds_st(lds + L_WO + (wave * 9 + j) * 1024 + lane * 16, ro[j]);
    take(IC<2>{}, lds + 2 * 40960); take(IC<3>{}, lds + 3 * 40960);
    WG_BAR();
    {
      u32x2 fr[5][4];
      const int gb = lane >> 4, q = (lane & 15) >> 2, pp = lane & 3;
#pragma unroll
      for (int j = 0; j < 5; ++j) {
        const int f = min(wave + 8 * j, 35);
#pragma unroll
        for (int a = 0; a < 4; ++a) {
          const int e = 16 * (gb >> 1) + 8 * (q >> 1) + 4 * (gb & 1) + 2 * (a & 1) + (q & 1) + 32 * (a >> 1);
          fr[j][a] = lds_tr(lds + L_WO + e * 1152 + (16 * f + 4 * pp) * 2);
        }
      }
#pragma unroll
      for (int j = 0; j < 5; ++j) { lds_st(lds + 2 * 40960 + chain_piece(wave, j) * 1024 + lane * 16, rw[0][j]); lds_st(lds + 3 * 40960 + chain_piece(wave, j) * 1024 + lane * 16, rw[1][j]); }
      WG_BAR();
#pragma unroll
      for (int j = 0; j < 5; ++j) {
        const int f = wave + 8 * j;
        if (f < 36) {
          lds_st(lds + L_WO + f * 2048 + lane * 16, u32x4{fr[j][0].x, fr[j][0].y, fr[j][1].x, fr[j][1].y});
          lds_st(lds + L_WO + f * 2048 + 1024 + lane * 16, u32x4{fr[j][2].x, fr[j][2].y, fr[j][3].x, fr[j][3].y});
        }
      }
    }
    take(IC<4>{}, lds + 2 * 40960); take(IC<5>{}, lds + 3 * 40960);
    WG_BAR();
#pragma unroll
    for (int j = 0; j < 5; ++j) { lds_st(lds + 2 * 40960 + chain_piece(wave, j) * 1024 + lane * 16, rw[2][j]); lds_st(lds + 3 * 40960 + chain_piece(wave, j) * 1024 + lane * 16, rw[3][j]); }
    WG_BAR();
    take(IC<6>{}, lds + 2 * 40960); take(IC<7>{}, lds + 3 * 40960);
    WG_BAR();
#pragma unroll
    for (int j = 0; j < 5; ++j) lds_st(lds + 2 * 40960 + chain_piece(wave, j) * 1024 + lane * 16, rw[4][j]);
    WG_BAR();
    take(IC<8>{}, lds + 2 * 40960);
    WG_BAR();
    if (!AW) {
#pragma unroll
      for (int j = 0; j < 3; ++j) {
        lds_st(lds + L_WV + ((wave & 3) * 3 + j) * 2048 + lane * 16, u32x4{tv[j][0], tv[j][1], tv[j][2], tv[j][3]});
        lds_st(lds + L_WV + ((wave & 3) * 3 + j) * 2048 + 1024 + lane * 16, u32x4{tv[j][4], tv[j][5], tv[j][6], tv[j][7]});
      }
    }
  }
  if (wave == 2) STAMP_AT(c, 1);
  const int myu = wave == 4 ? c : (wave == 5 && c < 8 ? 64 + c : -1);
  if (tid == 0) { lds_st(lds + L_CNT, 0u); lds_st(lds + L_CNT + 4, 0u); lds_st(lds + L_CNT + 8, 0u); }
  WG_BAR();
  unsigned pw = ex_parity(rW, lane);
  const unsigned go = myu >= 0 ? 72u * 16384u + ((unsigned)myu >> 6) * 16384u : 0u, gl = (myu & 63) * 128u;
  if (wave >= 6) {
#pragma unroll
    for (int j = 0; j < 5; ++j) {
      const int kb = (wave - 6) + 2 * j;
      if (kb < 9) {
        const int g = lane >> 4, nh = (lane >> 3) & 1, par = (lane >> 2) & 1, jj = lane & 3;
        const u16* src = p.mem0 + (2 * g + par) * 576 + 64 * kb + 8 * nh + 2 * jj;
        unsigned e[8];
#pragma unroll
        for (int t = 0; t < 8; ++t) e[t] = *(const GAS u16*)(src + 16 * (t & 3) + (t >> 2));
        lds_st(lds + L_AIM + kb * 1024 + (jj * 16 + 4 * g + 2 * nh + par) * 16, u32x4{e[0] | (e[1] << 16), e[2] | (e[3] << 16), e[4] | (e[5] << 16), e[6] | (e[7] << 16)});
      }
    }
  }
  chain_wait_tiles(rW, h, 1, lane);
  if (!AW) chain_tile_pieces(rW, lds, WS_QKV + h * 256, L_TILE, z, wave - 4, lane);
  WAIT_VM0();
  WG_BAR();
  if (wave == 4) { wait_ge(rW, WS_INI + lane * 128, true, 4u); wait_ge(rW, WS_INI + (64 + (lane & 15)) * 128, true, 4u); }
  lane = opaque(lane);
  const int idx = ((lane >> 1) & 1) ? 0xEEEE : 0x4444;
  const int i16 = lane & 15, g = lane >> 4;
  lptr aim = lds + L_AIM + lane * 16;
  lptr wof = lds + L_WO + lane * 16;
  const u32x4 rs4 = {UNI((unsigned)(unsigned long long)p.ws), UNI((unsigned)((unsigned long long)p.ws >> 32)), UNI((unsigned)(p.wsbytes > 0xFFFFFFF0ull ? 0xFFFFFFF0ull : p.wsbytes)), 0x00020000u};
  const unsigned vtro = (4 * g + (i16 >> 2)) * 128 + (((2 * wave + ((lane & 3) >> 1)) ^ ((((4 * g + (i16 >> 2)) >> 1) & 3) << 1)) << 4) + 8 * (lane & 1);
  const unsigned char* nextb = p.ws + WS_QKV + h * 256;
  Pause pp_ = {p.tune0, 0u}, pg = {p.tune1, 0u};
  const unsigned tl0 = (lane & 15) ^ (lane >> 4), tl1 = (lane >> 4) * 24576u, tl2 = (lane >> 3) * 24576u + (((lane & 7) ^ (((lane >> 4) & 3) << 1)) << 4);
  const unsigned pvo = (lane >> 3) * P_UNIT + (lane & 7) * 16;
  WG_BAR();

  for (int s = 0; s < NSEG; ++s) {
    TS(0);
    LST(0);
    STAMP_CLK(0);
    f32x4 aq = {0, 0, 0, 0}, ak = aq, av = aq;
    u32x4 kx[AW ? 8 : 1];
    constexpr int RG = 4;
#define NDA 5
    u32x4 a[RG];
    const bool dma = !AW && s + 1 < NSEG && !(p.tune2 & 2u);
    nextb += 32 * 24576;
    const unsigned tdst = L_TILE + ((s + 1) & 1) * TILE_B;
    {
#pragma unroll
      for (int kb = 0; kb < RG; ++kb) a[kb] = lds_ld<u32x4>(aim + kb * 1024);
      SCHED();
      lptr tb = lds + L_TILE + (s & 1) * TILE_B + T_K + i16 * 256;
      const int i16o = opaque(i16);
#pragma unroll
      for (int kb = 0; kb < 9; ++kb) {
        aq = smfmac(a[kb % RG], Wq[kb], aq, idx);
        ak = smfmac(a[kb % RG], Wk[kb], ak, idx);
        if (AW && kb < 2) av = smfmac(a[kb % RG], Wv[kb], av, idx);
        if (kb + RG < 9) a[kb % RG] = lds_ld<u32x4>(aim + (kb + RG) * 1024);
        if (!AW && kb < NDA && dma) chain_tile_dma(nextb, tdst, z, wave - 4, kb, tl0, tl1, tl2);
        if (AW && kb >= 5) { const int e = kb - 5; const unsigned o = ((4 * e + g) ^ i16o) << 4; kx[2 * e] = lds_ld<u32x4>(tb + o); kx[2 * e + 1] = lds_ld<u32x4>(tb + 4096 + o); }
        SCHED();
      }
    }
    {
      const unsigned qp = pk2(aq[0] + aq[2], aq[1] + aq[3]), kp = pk2(ak[0] + ak[2], ak[1] + ak[3]);
      lptr qw = lds + L_QIM + 2 * g * 272 + (16 * wave + i16) * 2;
      lds_st(qw, (u16)qp); lds_st(qw + 272, (u16)(qp >> 16));
      lds_st(qw + (L_KIM - L_QIM), (u16)kp); lds_st(qw + (L_KIM - L_QIM) + 272, (u16)(kp >> 16));
    }
    TS(1);
    WG_BAR();
    TS(2);
    if (AW) __builtin_amdgcn_s_setprio(1);
    const unsigned sp0 = UNI(ex_span(exoff, pw, s));
    const unsigned pb = sp0 + c * 128;
    if (!AW) {
      if (!(p.tune2 & 32u)) __builtin_amdgcn_s_sleep(2);
      {
        const u32x8 x0 = chain_wo_frag(lds + L_WV + ((wave & 3) * 3) * 2048 + lane * 16);
        const u32x4 b2 = lds_ld<u32x4>(aim + 2 * 1024), b3 = lds_ld<u32x4>(aim + 3 * 1024), b4 = lds_ld<u32x4>(aim + 4 * 1024);
        const u32x8 x1 = chain_wo_frag(lds + L_WV + ((wave & 3) * 3 + 1) * 2048 + lane * 16);
        SCHED();
        av = smfmac(a[1], Wv[3], av, idx);
        av = smfmac(a[2], x0, av, idx);
        SCHED();
        const u32x8 x2 = chain_wo_frag(lds + L_WV + ((wave & 3) * 3 + 2) * 2048 + lane * 16);
        SCHED();
        av = smfmac(b2, Wv[0], av, idx);
        av = smfmac(b3, Wv[1], av, idx);
        av = smfmac(b4, Wv[2], av, idx);
        av = smfmac(a[3], x1, av, idx);
        av = smfmac(a[0], x2, av, idx);
      }
      lds_st(lds + L_VPX + (wave - 4) * 512 + lane * 8, f32x2{av[0] + av[2], av[1] + av[3]});
      if (dma) {
#pragma unroll
        for (int i = NDA; i < 5; ++i) chain_tile_dma(nextb, tdst, z, wave - 4, i, tl0, tl1, tl2);
      }
      if (wave == 5 && s > 1) chain_raise(p, c, (unsigned)s - 1u, lane);
      TS(3);
      WG_BAR();
      TS(4);
      SCHED();
      const u32x8 w0 = chain_wo_frag(wof + (wave * 4 + 0) * 2048), w1 = chain_wo_frag(wof + (wave * 4 + 1) * 2048),
                  w2 = chain_wo_frag(wof + (wave * 4 + 2) * 2048), w3 = chain_wo_frag(wof + (wave * 4 + 3) * 2048);
      f32x4 d0 = {0, 0, 0, 0}, d1 = d0;
      asm volatile("" : "+v"(d0), "+v"(d1));
      SCHED();
      if (wave >= 6 && s > 0) chain_store_ox(rW, lds, h, z, s - 1, wave - 6, lane);
      SCHED();
      const unsigned pu = UNI(sp0 + myu * P_UNIT), pu4 = UNI(pu + 4096);
      const unsigned gw = UNI(sp0 + go + gl);
      SCHED();
      TS(5);
      WG_BAR();
      TS(6);
      __builtin_amdgcn_s_setprio(2);
      LST(1);
      if (s + 1 == NSEG && wave >= 6) chain_publish_ox(p, lds, c, (unsigned)s, lane);
      if (s + 1 < NSEG) {
        {
          const u32x4 oa = lds_ld<u32x4>(lds + L_OIM + lane * 16);
          d0 = smfmac(oa, w0, d0, idx);
          d1 = smfmac(oa, w1, d1, idx);
          const f32x4 d2 = smfmac(oa, w2, f32x4{0, 0, 0, 0}, idx), d3 = smfmac(oa, w3, f32x4{0, 0, 0, 0}, idx);
          st16c(rW, pvo, pb + 8 * wave * P_UNIT, u32x4{chain_pk_tile(d0), chain_pk_tile(d1), chain_pk_tile(d2), chain_pk_tile(d3)});
        }
        TS(7);
        if (myu >= 0) {
          for (unsigned i = 0; i < pp_.d; ++i) __builtin_amdgcn_s_sleep(1);
          ACLK(tpi); ACC(5, tpi, tb2);
          LST(2);
          TS(8);
          f32x4 a0; unsigned gvo;
          const unsigned lo16 = opaque(lane) * 16;
          unsigned sp = 0;
          for (;; ++sp) {
            MEMBAR();
            u32x4 v[8];
#pragma unroll
            for (int j = 0; j < 8; ++j) v[j] = ld16c(rW, lo16 + (j & 3) * 1024, j < 4 ? pu : pu4);
            u32x4 sel;
            {
              const int l = opaque(lane), i = l & 15, jj = 2 * (i & 3) + ((i >> 2) & 1);
              const unsigned one = i < 8 ? ((jj & 1) ? 0x3F800000u : 0x00003F80u) : 0u; const int w = jj >> 1;
              sel = u32x4{w == 0 ? one : 0u, w == 1 ? one : 0u, w == 2 ? one : 0u, w == 3 ? one : 0u};
              gvo = (l < 32 && i < 8) ? (l >> 4) * 64 + i * 8 : 0x80000000u;
            }
            SCHED();
            a0 = mfma16(sel, v[0], f32x4{0, 0, 0, 0});
#pragma unroll
            for (int j = 1; j < 8; ++j) a0 = mfma16(sel, v[j], a0);
            if (__builtin_expect(!__any(a0[0] != a0[0]), 1)) break;
            asm volatile("" : "+v"(v[0]), "+v"(v[1]), "+v"(v[2]), "+v"(v[3]), "+v"(v[4]), "+v"(v[5]), "+v"(v[6]), "+v"(v[7]));
            unsigned mx = 0;
#pragma unroll
            for (int j = 0; j < 8; ++j) mx = max(mx, umax4(v[j]));
            if (__all(mx != EMPTY)) break;
            if ((sp & 63u) == 63u) {
              if (__builtin_amdgcn_readfirstlane(ld4c(rW, WS_ABORT)) != 0u) break;
              if (sp > SPIN_CAP) { st4c(rW, WS_ABORT, 0, 2u); break; }
            }
          }
          pause_adapt(pp_, p.tune0, sp == 0, p.tune2);
          ACLK(tps); ACC(1, tps, tb2);
          LST(3);
          TS(9);
#pragma unroll
          for (int r = 0; r < 4; ++r) a0[r] += dppf<0x128>(a0[r]);
          const u32x2 o = {min(pk2(a0[0], a0[1]), 0xFFFEFFFFu), min(pk2(a0[2], a0[3]), 0xFFFEFFFFu)};
          st8c(rW, gvo, gw, o);
          if (wave == 4) lds_st(lds + L_CNT + 8, (unsigned)s + 1u);
          ACLK(tgs); ACC(2, tgs, tb2);
          LST(4);
          TS(10);
        }
        chain_gather(p, rW, lds, s, wave == 6 ? 4 : (wave == 7 ? 6 : wave + 3), wave == 6, sp0 + 72u * P_UNIT, pg, lane GATHER_DBG);
      }
    } else {
      f32x4 s0 = {0, 0, 0, 0}, s1 = s0, s2 = s0;
      {
        lptr qb = lds + L_QIM + (i16 & 7) * 272 + g * 16, kb_ = lds + L_KIM + (2 * (i16 >> 2) + (i16 & 1)) * 272 + g * 16;
        u32x4 bq[4], am[4];
#pragma unroll
        for (int e = 0; e < 4; ++e) bq[e] = lds_ld<u32x4>(qb + e * 64);
        am[0] = lds_ld<u32x4>(kb_); am[1] = lds_ld<u32x4>(kb_ + 64);
        SCHED();
        s1 = mfma16(kx[0], bq[0], s1); s2 = mfma16(kx[1], bq[0], s2);
        s1 = mfma16(kx[2], bq[1], s1); s2 = mfma16(kx[3], bq[1], s2);
        am[2] = lds_ld<u32x4>(kb_ + 128); am[3] = lds_ld<u32x4>(kb_ + 192);
        SCHED();
        s0 = mfma16(am[0], bq[0], s0); s1 = mfma16(kx[4], bq[2], s1); s2 = mfma16(kx[5], bq[2], s2);
        s0 = mfma16(am[1], bq[1], s0); s1 = mfma16(kx[6], bq[3], s1); s2 = mfma16(kx[7], bq[3], s2);
        s0 = mfma16(am[2], bq[2], s0); s0 = mfma16(am[3], bq[3], s0);
      }
      STAMP_CLK(3);
      lptr tv = lds + L_TILE + (s & 1) * TILE_B + T_V + vtro;
      const u32x2 va0 = lds_tr(tv), va1 = lds_tr(tv + 16 * 128);
      const u32x8 w0 = chain_wo_frag(wof + (wave * 4 + 0) * 2048), w1 = chain_wo_frag(wof + (wave * 4 + 1) * 2048),
                  w2 = chain_wo_frag(wof + (wave * 4 + 2) * 2048), w3 = chain_wo_frag(wof + (wave * 4 + 3) * 2048);
      SCHED();
      float v[10] = {s0[0], s0[1], s1[0], s1[1], s1[2], s1[3], s2[0], s2[1], s2[2], s2[3]};
      float mx = fmax2(fmax2(fmax2(v[0], v[1]), fmax2(v[2], v[3])), fmax2(fmax2(fmax2(v[4], v[5]), fmax2(v[6], v[7])), fmax2(v[8], v[9])));
      mx = xmax32(xmax16(mx));
      const float nm = -mx * (SCALE * LOG2E);
#pragma unroll
      for (int r = 0; r < 10; ++r) v[r] = __builtin_amdgcn_exp2f(__builtin_fmaf(v[r], SCALE * LOG2E, nm));
      const u32x4 pt = {pk2(v[2], v[3]), pk2(v[4], v[5]), pk2(v[6], v[7]), pk2(v[8], v[9])};
      const u32x4 pm = {pk2(v[0], v[1]), 0u, 0u, 0u};
      SCHED();
      TS(3);
      RAW_BAR();
      TS(4);
      const f32x2 vp = lds_ld<f32x2>(lds + L_VPX + wave * 512 + lane * 8);
      SCHED();
      f32x4 o = mfma16(u32x4{va0.x, va0.y, va1.x, va1.y}, pt, f32x4{0, 0, 0, 0});
      float sum = ((v[0] + v[1]) + (v[2] + v[3])) + ((v[4] + v[5]) + (v[6] + v[7])) + (v[8] + v[9]);
      sum = xr32(xr16(sum, false), false);
      const float inv = __builtin_amdgcn_rcpf(sum);
      SCHED();
      const unsigned vm = pk2(av[0] + av[2] + vp[0], av[1] + av[3] + vp[1]);
      o = mfma16(u32x4{vm, 0u, 0u, 0u}, pm, o);
      lds_st(lds + L_VIM + wave * 256 + lane * 4, vm);
      if (i16 < 8) lds_st(lds + L_OIM + (wave * 16 + 4 * (i16 >> 1) + 2 * (g >> 1) + (i16 & 1)) * 16 + (g & 1) * 8, u32x2{pk2(o[0] * inv, o[1] * inv), pk2(o[2] * inv, o[3] * inv)});
      STAMP_CLK(5);
      TS(5);
      WG_BAR();
      TS(6);
      LST(1);
      STAMP_CLK(6);
      if (s + 1 < NSEG) {
        const u32x4 oa = lds_ld<u32x4>(lds + L_OIM + lane * 16);
        const f32x4 z4 = {0, 0, 0, 0};
        if (wave == 0) {
          const u32x8 x0 = chain_wo_frag(wof + 32 * 2048), x1 = chain_wo_frag(wof + 33 * 2048);
          SCHED();
          const f32x4 d0 = smfmac(oa, w0, z4, idx), d1 = smfmac(oa, w1, z4, idx);
          SCHED();
          const u32x8 x2 = chain_wo_frag(wof + 34 * 2048), x3 = chain_wo_frag(wof + 35 * 2048);
          SCHED();
          const f32x4 d2 = smfmac(oa, w2, z4, idx), d3 = smfmac(oa, w3, z4, idx);
          st16c(rW, pvo, pb, u32x4{chain_pk_tile(d0), chain_pk_tile(d1), chain_pk_tile(d2), chain_pk_tile(d3)});
          SCHED();
          const f32x4 e0 = smfmac(oa, x0, z4, idx), e1 = smfmac(oa, x1, z4, idx), e2 = smfmac(oa, x2, z4, idx), e3 = smfmac(oa, x3, z4, idx);
          st16c(rW, pvo, pb + 64 * P_UNIT, u32x4{chain_pk_tile(e0), chain_pk_tile(e1), chain_pk_tile(e2), chain_pk_tile(e3)});
        } else {
          const f32x4 d0 = smfmac(oa, w0, z4, idx), d1 = smfmac(oa, w1, z4, idx), d2 = smfmac(oa, w2, z4, idx), d3 = smfmac(oa, w3, z4, idx);
          st16c(rW, pvo, pb + 8 * wave * P_UNIT, u32x4{chain_pk_tile(d0), chain_pk_tile(d1), chain_pk_tile(d2), chain_pk_tile(d3)});
        }
      }
      TS(7);
      LST(2);
      if (wave < 2) __builtin_amdgcn_s_setprio(0); else __builtin_amdgcn_s_setprio(2);
      if (wave < 2) {
        if (!(p.tune2 & 1u)) chain_token_attn(lds, s, wave, lane);
        LST(3);
        if (s + 1 == NSEG) { asm volatile("s_waitcnt lgkmcnt(0)" ::: "memory"); chain_store_ox(rW, lds, h, z, s, wave, lane); chain_publish_ox(p, lds + 4, c, NSEG, lane); }
      } else if (s + 1 < NSEG) {
        if (wave == 2 && s + 2 < NSEG && (s < 2 || ((s + 2) & 3) == 0)) chain_wait_tiles(rW, h, s < 2 ? s + 2 : s + 5, lane);
        if (wave == 3 && s + 2 < NSEG && (p.tune2 & 16u) == 0u) { const unsigned t = ld4c(rW, 0, UNI(exoff + (unsigned)(s + 1) * EX_STEP + 100u * 16384u)); asm volatile("" :: "v"(t)); }
        if (((s & 3) == 3 || s == 1) && wave == 3) {
          const int l = opaque(lane); const unsigned need = min(s + 9, NSEG - 1);
          wait_ge(rW, WS_INI + l * 128, true, need); wait_ge(rW, WS_INI + (64 + (l & 15)) * 128, true, need);
        }
        chain_gather(p, rW, lds, s, wave == 2 ? 0 : 2, true, sp0 + 72u * P_UNIT, pg, lane GATHER_DBG);
      }
    }
    LST(5);
    TS(14);

    WG_BAR();
    __builtin_amdgcn_s_setprio(0);
    STAMP_CLK(9);
  }
  if (wave == 2) STAMP_AT(c, 66);
}

__device__ __forceinline__ unsigned wave_min(unsigned v) {
  v = min(v, (unsigned)__builtin_amdgcn_update_dpp(0xffffffff, v, 0xB1, 0xf, 0xf, false)); v = min(v, (unsigned)__builtin_amdgcn_update_dpp(0xffffffff, v, 0x4E, 0xf, 0xf, false));
  v = min(v, (unsigned)__builtin_amdgcn_update_dpp(0xffffffff, v, 0x141, 0xf, 0xf, false)); v = min(v, (unsigned)__builtin_amdgcn_update_dpp(0xffffffff, v, 0x140, 0xf, 0xf, false));
  { const auto r = __builtin_amdgcn_permlane16_swap(v, v, false, false); v = min((unsigned)r[0], (unsigned)r[1]); }
  { const auto r = __builtin_amdgcn_permlane32_swap(v, v, false, false); v = min((unsigned)r[0], (unsigned)r[1]); }
  return v;
}
__device__ __forceinline__ unsigned wait_known(rsrc_t rW, lptr slot, unsigned off, bool act, unsigned target) {
  if (threadIdx.x < 64) {
    const unsigned v = wave_min(wait_ge<1>(rW, off, act, target) | (act ? 0u : 0xffffffffu));
    if (threadIdx.x == 0) lds_st(slot, max(v, target));
  }
  WG_BAR();
  return UNI(lds_ld<unsigned>(slot));
}
__device__ __forceinline__ unsigned peek_issue(rsrc_t rW, unsigned off) { return ld4c(rW, threadIdx.x < 64 ? off : 0x80000000u); }
__device__ __forceinline__ void peek_finish(lptr slot, unsigned v) {
  if (threadIdx.x < 64) { v = wave_min(v); if (threadIdx.x == 0) lds_st(slot, v); }
}
__device__ __forceinline__ void ex_mark(rsrc_t rW, unsigned exoff, unsigned pw, int i80, int s) {
  const int lane = threadIdx.x & 63, wave = __builtin_amdgcn_readfirstlane(threadIdx.x >> 6);
  const unsigned em = (unsigned)opaque(-1), k = i80 * 8 + wave;
  if (k < 592 && s < NSEG - 1) st16c(rW, lane * 16 + (k & 7) * 1024, UNI(ex_span(exoff, pw, s) + (k >> 3) * 16384), u32x4{em, em, em, em});
}
__device__ __forceinline__ void publish_wg(rsrc_t rW, int role, unsigned val) {
  WAIT_VM0();
  WG_BAR();
  if (threadIdx.x == 0) st4c(rW, WS_PROG + role * 128, 0, val);
}

__device__ __forceinline__ void prologue(const Params& p, rsrc_t rW, lptr lds, int i80) {
  const int tid = threadIdx.x, lane = tid & 63, wave = __builtin_amdgcn_readfirstlane(tid >> 6);
  u32x4 xa[4], xb[4];
#pragma unroll
  for (int j = 0; j < 4; ++j) {
    const int row = min((j * 80 + i80) * 8 + wave, S_ - 1);
    xa[j] = *(const GAS u32x4*)(p.x + (size_t)row * 576 + 8 * lane);
    xb[j] = *(const GAS u32x4*)(p.x + (size_t)row * 576 + 512 + 8 * (lane & 7));
  }
  if (i80 < 54) {
    const int kg = tid >> 3, cg = tid & 7;
    u32x4 w[9]; float sc[9];
#pragma unroll
    for (int i = 0; i < 9; ++i) { w[i] = *(const GAS u32x4*)(p.wmod + (size_t)(kg + 64 * i) * 3456 + 64 * i80 + 8 * cg); const float v = bf2f(p.c[kg + 64 * i]); sc[i] = rbf(v / (1.0f + __expf(-v))); }
    float acc[8];
#pragma unroll
    for (int j = 0; j < 8; ++j) acc[j] = 0.f;
#pragma unroll
    for (int i = 0; i < 9; ++i) {
      acc[0] += sc[i] * blo(w[i].x); acc[1] += sc[i] * bhi(w[i].x); acc[2] += sc[i] * blo(w[i].y); acc[3] += sc[i] * bhi(w[i].y);
      acc[4] += sc[i] * blo(w[i].z); acc[5] += sc[i] * bhi(w[i].z); acc[6] += sc[i] * blo(w[i].w); acc[7] += sc[i] * bhi(w[i].w);
    }
#pragma unroll
    for (int j = 0; j < 8; ++j) {
      float v = acc[j];
      v += __shfl_xor(v, 8); v += __shfl_xor(v, 16); v += __shfl_xor(v, 32);
      if (lane < 8) lds_st(lds + (wave * 64 + 8 * cg + j) * 4, v);
    }
    WG_BAR();
    if (tid < 64) {
      float v = 0.f;
#pragma unroll
      for (int w8 = 0; w8 < 8; ++w8) v += lds_ld<float>(lds + (w8 * 64 + tid) * 4);
      st4c(rW, tid * 4, UNI(WS_MOD + i80 * 256), __builtin_bit_cast(unsigned, rbf(v)));
      WAIT_VM0();
      st4c(rW, tid == 0 ? WS_MODP + i80 * 128 : 0x80000000u, 0, 1u);
    }
  }
  wait_ge(rW, WS_MODP + (lane < 18 ? lane : 0) * 128, lane < 18, 1u);
  float c1[16], sa[16];
#pragma unroll
  for (int j = 0; j < 4; ++j) {
    const unsigned col = (j < 2 ? 8 * lane + 4 * j : 512 + 8 * (lane & 7) + 4 * (j - 2)) * 4;
    const f32x4 a = __builtin_bit_cast(f32x4, ld16c(rW, col, WS_MOD)), b = __builtin_bit_cast(f32x4, ld16c(rW, col, WS_MOD + 576 * 4));
#pragma unroll
    for (int k = 0; k < 4; ++k) { sa[4 * j + k] = a[k]; c1[4 * j + k] = rbf(1.0f + b[k]); }
  }
#pragma unroll
  for (int j = 0; j < 4; ++j) {
    const int grp = j * 80 + i80;
    if (grp < 256) {
      float v[16];
      unpack8(xa[j], v); unpack8(xb[j], v + 8);
      u32x4 o0, o1;
      adaln_row_r(v, lane, c1, sa, o0, o1);
      const unsigned ho = UNI(WS_H + (unsigned)(grp * 8 + wave) * 1152u);
      st16c(rW, lane * 16, ho, o0);
      st16c(rW, lane < 8 ? 1024 + lane * 16 : 0x80000000u, ho, o1);
      WAIT_VM0();
      WG_BAR();
      if (tid == 0) st4c(rW, WS_HP + i80 * 128, 0, (unsigned)j + 1u);
    }
  }
}

__device__ __forceinline__ void t4_role(const Params& p, rsrc_t rW, lptr lds, int a, unsigned exoff) {
  const int tid = threadIdx.x, lane = tid & 63, wave = __builtin_amdgcn_readfirstlane(tid >> 6);
  const int ng = a % 9, kq = a / 9, nt = wave & 1, kr = wave >> 1;
  const u32x4 rs = mk_rs4(p.ws, (unsigned)(p.wsbytes > 0xFFFFFFF0ull ? 0xFFFFFFF0ull : p.wsbytes));
  if (p.rolemask & 64u) { prologue(p, rW, lds, a); WG_BAR(); }
  const bool exi = p.rolemask & 1u;
  if (exi) ex_init(rW, exoff, 4u, a, 0, 4);
  u32x4 W[16];
  {
    const u16* src = p.wo + (size_t)(kq * 1024 + (lane >> 3)) * 576 + ng * 64 + (lane & 7) * 8;
#pragma unroll
    for (int j = 0; j < 16; ++j) glds16(src + (size_t)(wave * 16 + j) * 8 * 576, (wave * 16 + j) * 1024);
    WAIT_VM0();
    WG_BAR();
    const int G = lane >> 4, q = (lane & 15) >> 2, pp = lane & 3;
#pragma unroll
    for (int ks = 0; ks < 16; ++ks) {
      const int row = kr * 256 + 16 * ks + 8 * (G >> 1) + q, col = nt * 32 + 16 * (G & 1) + 4 * pp;
      const u32x2 a0 = lds_tr(lds + row * 128 + col * 2), a1 = lds_tr(lds + (row + 4) * 128 + col * 2);
      W[ks] = u32x4{a0.x, a0.y, a1.x, a1.y};
    }
    WG_BAR();
  }
  const unsigned pw = exi ? ex_parity(rW, lane) : 0u;
  if (exi) ex_init(rW, exoff, pw, a, 4, 16);
  auto fetch = [&](int s) {
#pragma unroll
    for (int j = 0; j < 8; ++j) {
      const unsigned row = wave * 4 + (j >> 1);
      blds16c(rs, ((lane ^ (row & 15)) << 4) + (j & 1) * 1024, UNI(WS_OX + ((unsigned)s * 32u + row) * 8192u + kq * 2048u), UNI((s & 1) * 65536 + (wave * 8 + j) * 1024));
    }
  };
  auto announce = [&](int done) {
    if (tid == 0) {
      st4c(rW, WS_PROG + (R_T4 + a) * 128, 0, (unsigned)done);
      if (exi) st4c(rW, WS_INI + a * 128, 0, (unsigned)min(done + 16, NSEG - 1));
      STAMP_AT(R_T4 + a, done - 1);
    }
  };
  unsigned known = 0;
  bool ahead = false, owed = false;
  lptr kslot = lds + 131072;
  for (int s = 0; s < NSEG; ++s) {
    if (!ahead) {
      if (known < (unsigned)s + 1u) known = wait_known(rW, kslot + 8 + (s & 1) * 4, WS_PROG + (R_CHAIN + kq * 16 + (lane & 15)) * 128, true, (unsigned)s + 1u);
      fetch(s);
    }
    if (s == 30 && tid == 0) STAMP_AT(R_T4 + a, 200);
    WAIT_VM0();
    WG_BAR();
    if (owed) announce(s);
    ahead = s + 1 < NSEG && known >= (unsigned)s + 2u;
    if (ahead) fetch(s + 1);
    unsigned pk = 0;
    if (!ahead) pk = peek_issue(rW, WS_PROG + (R_CHAIN + kq * 16 + (lane & 15)) * 128);
    if (s == 30 && tid == 0) STAMP_AT(R_T4 + a, 201);
    lptr tb = lds + (s & 1) * 65536;
    f32x16 acc;
#pragma unroll
    for (int r = 0; r < 16; ++r) acc[r] = 0.f;
    {
      const unsigned row = lane & 31, hh = lane >> 5;
      u32x4 bt[16];
#pragma unroll
      for (int ks = 0; ks < 16; ++ks) { const unsigned ch = kr * 32 + 2 * ks + hh; bt[ks] = lds_ld<u32x4>(tb + row * 2048 + ((ch ^ (row & 15)) << 4)); }
      SCHED();
#pragma unroll
      for (int ks = 0; ks < 16; ++ks) acc = mfma32(W[ks], bt[ks], acc);
      SCHED();
      if (!ahead) peek_finish(kslot + (s & 1) * 4, pk);
      WG_BAR();
      if (!ahead && s + 1 < NSEG) {
        known = max(known, UNI(lds_ld<unsigned>(kslot + (s & 1) * 4)));
        if (known >= (unsigned)s + 2u) { ahead = true; fetch(s + 1); }
      }
      lptr red = tb + (kr * 2 + nt) * 4608 + row * 144 + hh * 16;
#pragma unroll
      for (int g = 0; g < 4; ++g) lds_st(red + g * 32, f32x4{acc[4 * g], acc[4 * g + 1], acc[4 * g + 2], acc[4 * g + 3]});
    }
    WG_BAR();
    if (s == 30 && tid == 0) STAMP_AT(R_T4 + a, 202);
    {
      const unsigned tok = tid >> 4, c4 = tid & 15;
      lptr red = tb + (c4 >> 3) * 4608 + tok * 144 + (c4 & 7) * 16;
      const f32x4 v = (lds_ld<f32x4>(red) + lds_ld<f32x4>(red + 2 * 4608)) + (lds_ld<f32x4>(red + 4 * 4608) + lds_ld<f32x4>(red + 6 * 4608));
      st16c(rW, tok * 2304u + ng * 256u + c4 * 16, UNI(WS_PT + (unsigned)(s * 4 + kq) * 73728u), __builtin_bit_cast(u32x4, v));
    }
    if (exi) ex_mark(rW, exoff, pw, a, s + 16);
    owed = ahead;
    if (!ahead) {
      peek_finish(kslot + 16 + (s & 1) * 4, peek_issue(rW, WS_PROG + (R_CHAIN + kq * 16 + (lane & 15)) * 128));
      WAIT_VM0(); WG_BAR(); announce(s + 1);
      known = max(known, UNI(lds_ld<unsigned>(kslot + 16 + (s & 1) * 4)));
    }
  }
}

__device__ __forceinline__ void ln_role(const Params& p, rsrc_t rW, lptr lds, int r8, unsigned exoff) {
  const int tid = threadIdx.x, lane = tid & 63, wave = __builtin_amdgcn_readfirstlane(tid >> 6);
  if (p.rolemask & 64u) { prologue(p, rW, lds, 72 + r8); WG_BAR(); }
  if (p.rolemask & 1u) { ex_init(rW, exoff, 4u, 72 + r8, 0, 4); const unsigned pw = ex_parity(rW, lane); ex_init(rW, exoff, pw, 72 + r8, 4, NSEG - 1); }
  if (tid < 4) lds_st(lds + 256 + tid * 4, 0u);
  WG_BAR();
  const int r4 = wave >> 1, hf = wave & 1;
  const bool act = lane < 36;
  const unsigned colb = (unsigned)(hf * 288 + 8 * (act ? lane : 0));
  float ga[8], c1[8], sb[8];
  if (p.rolemask & 64u) wait_ge(rW, WS_MODP + (lane < 54 ? lane : 0) * 128, lane < 54, 1u);
#pragma unroll
  for (int j = 0; j < 2; ++j) {
    const unsigned col = (colb + 4 * j) * 4;
    const f32x4 a = __builtin_bit_cast(f32x4, ld16c(rW, col, WS_MOD + 2 * 576 * 4)), b = __builtin_bit_cast(f32x4, ld16c(rW, col, WS_MOD + 4 * 576 * 4)), c = __builtin_bit_cast(f32x4, ld16c(rW, col, WS_MOD + 3 * 576 * 4));
#pragma unroll
    for (int k = 0; k < 4; ++k) { ga[4 * j + k] = a[k]; c1[4 * j + k] = rbf(1.0f + b[k]); sb[4 * j + k] = c[k]; }
  }
  for (int s = 0; s < NSEG; ++s) {
    const int row = s * 32 + r8 * 4 + r4;
    const u32x4 xa = *(const GAS u32x4*)(p.x + (size_t)row * 576 + colb);
    wait_ge(rW, WS_PROG + (R_T4 + (lane < 36 ? lane : 0)) * 128, lane < 36, (unsigned)s + 1u);
    if (s == 30 && wave == 0) STAMP_AT(R_LN + r8, 200);
    f32x4 t[4][2];
    const unsigned so = UNI(WS_PT + (unsigned)s * 4u * 73728u + (r8 * 4 + r4) * 2304u);
#pragma unroll
    for (int k = 0; k < 4; ++k) {
      t[k][0] = __builtin_bit_cast(f32x4, ld16c(rW, colb * 4, so + k * 73728u));
      t[k][1] = __builtin_bit_cast(f32x4, ld16c(rW, colb * 4 + 16, so + k * 73728u));
    }
    float v[8];
    unpack8(xa, v);
    float sm = 0.f;
#pragma unroll
    for (int j = 0; j < 8; ++j) {
      const float att = (t[0][j >> 2][j & 3] + t[1][j >> 2][j & 3]) + (t[2][j >> 2][j & 3] + t[3][j >> 2][j & 3]);
      v[j] = rbf(v[j] + rbf(ga[j] * rbf(att)));
      sm += v[j];
    }
    if (s == 30 && wave == 0) STAMP_AT(R_LN + r8, 201);
    const unsigned vo = act ? colb * 2 : 0x80000000u;
    st16c(rW, vo, UNI(WS_X1 + (unsigned)row * 1152u), u32x4{pk2(v[0], v[1]), pk2(v[2], v[3]), pk2(v[4], v[5]), pk2(v[6], v[7])});
    lptr ex = lds + (s & 1) * 128 + r4 * 16;
    sm = wave_sum(act ? sm : 0.f);
    if (lane == 0) lds_st(ex + hf * 4, sm);
    WG_BAR();
    const f32x2 m2 = lds_ld<f32x2>(ex);
    const float mean = (m2[0] + m2[1]) * (1.0f / 576.0f);
    float q = 0.f;
#pragma unroll
    for (int j = 0; j < 8; ++j) { const float d = v[j] - mean; q += d * d; }
    q = wave_sum(act ? q : 0.f);
    if (lane == 0) lds_st(ex + 8 + hf * 4, q);
    WG_BAR();
    const f32x2 q2 = lds_ld<f32x2>(ex + 8);
    const float var = (q2[0] + q2[1]) * (1.0f / 576.0f);
    const float mb = rbf(mean), rs = __builtin_amdgcn_rcpf(rbf(__builtin_amdgcn_sqrtf(rbf(rbf(var) + 1e-5f))));
    float r[8];
#pragma unroll
    for (int j = 0; j < 8; ++j) r[j] = rbf(rbf(rbf(v[j] - mb) * rs) * c1[j]) + sb[j];
    if (s == 30 && wave == 0) STAMP_AT(R_LN + r8, 202);
    st16c(rW, vo, UNI(WS_H2 + (unsigned)row * 1152u), u32x4{pk2(r[0], r[1]), pk2(r[2], r[3]), pk2(r[4], r[5]), pk2(r[6], r[7])});
    WAIT_VM0();
    unsigned old = 0;
    if (lane == 0) old = __hip_atomic_fetch_add((LAS unsigned*)(lds + 256 + r4 * 4), 1u, __ATOMIC_RELAXED, __HIP_MEMORY_SCOPE_WORKGROUP);
    old = __builtin_amdgcn_readfirstlane(old);
    if (old & 1u) st4c(rW, lane == 0 ? WS_LNP + (r8 * 4 + r4) * 128 : 0x80000000u, 0, (unsigned)s + 1u);
    if (wave == 0) STAMP_AT(R_LN + r8, s);
  }
}

__device__ __forceinline__ float gelu_tanh(float x) {
  const float u = 0.7978845608028654f * (x + 0.044715f * x * x * x);
  const float e = __builtin_amdgcn_exp2f(u * (2.0f * LOG2E));
  const float th = 1.0f - 2.0f * __builtin_amdgcn_rcpf(e + 1.0f);
  return 0.5f * x * (1.0f + th);
}

__device__ __forceinline__ void proj_core(lptr hb, const u32x4 (&W)[18], f32x4 (&acc)[2], int lane) {
  const int i16 = lane & 15, g = lane >> 4;
  const unsigned sw = (i16 >> 1) & 7;
  lptr r0 = hb + i16 * 1152 + ((g ^ sw) << 4), r1 = hb + i16 * 1152 + (((g + 4) ^ sw) << 4);
  u32x4 b[2][12];
  auto rd = [&](int bi, int k0) {
#pragma unroll
    for (int k = 0; k < 6; ++k) {
      const int ks = k0 + k;
      lptr a = ((ks & 1) ? r1 : r0) + (ks >> 1) * 128;
      b[bi][2 * k] = lds_ld<u32x4>(a); b[bi][2 * k + 1] = lds_ld<u32x4>(a + 16 * 1152);
    }
  };
  auto mm = [&](int bi, int k0) {
#pragma unroll
    for (int k = 0; k < 6; ++k) { acc[0] = mfma16(W[k0 + k], b[bi][2 * k], acc[0]); acc[1] = mfma16(W[k0 + k], b[bi][2 * k + 1], acc[1]); }
  };
  rd(0, 0); rd(1, 6);
  SCHED();
  mm(0, 0);
  SCHED();
  rd(0, 12);
  SCHED();
  mm(1, 6);
  SCHED();
  mm(0, 12);
}

__device__ __forceinline__ void t5_role(const Params& p, rsrc_t rW, lptr lds, int b, unsigned exoff) {
  const int tid = threadIdx.x, lane = tid & 63, wave = __builtin_amdgcn_readfirstlane(tid >> 6);
  if (p.rolemask & 64u) { prologue(p, rW, lds, 36 + b); WG_BAR(); }
  const u32x4 rs = mk_rs4(p.ws, (unsigned)(p.wsbytes > 0xFFFFFFF0ull ? 0xFFFFFFF0ull : p.wsbytes));
  const bool exi = p.rolemask & 1u;
  if (exi) ex_init(rW, exoff, 4u, 36 + b, 0, 4);
  u32x4 W[18];
  {
    const u16* src = p.win + (size_t)(lane >> 4) * F_ + b * 128 + (lane & 15) * 8;
#pragma unroll
    for (int j = 0; j < 18; ++j) glds16(src + (size_t)(wave * 18 + j) * 4 * F_, (wave * 18 + j) * 1024);
    WAIT_VM0();
    WG_BAR();
    const int g = lane >> 4, q = (lane & 15) >> 2, pp = lane & 3;
#pragma unroll
    for (int ks = 0; ks < 18; ++ks) {
      const int row = 32 * ks + 8 * g + q, col = wave * 16 + 4 * pp;
      const u32x2 a0 = lds_tr(lds + row * 256 + col * 2), a1 = lds_tr(lds + (row + 4) * 256 + col * 2);
      W[ks] = u32x4{a0.x, a0.y, a1.x, a1.y};
    }
    WG_BAR();
  }
  unsigned so[5];
#pragma unroll
  for (int j = 0; j < 5; ++j) {
    const unsigned L = (wave + 8 * j) * 1024 + lane * 16, row = L / 1152u, ch = (L % 1152u) >> 4;
    so[j] = row * 1152u + ((ch ^ ((row >> 1) & 7)) << 4);
  }
  const int i16 = lane & 15, g = lane >> 4;
  const unsigned pw = exi ? ex_parity(rW, lane) : 0u;
  if (exi) ex_init(rW, exoff, pw, 36 + b, 4, 16);
  auto fetch = [&](int s) {
#pragma unroll
    for (int j = 0; j < 5; ++j) if (wave + 8 * j < 36) blds16c(rs, so[j], UNI(WS_H2 + (unsigned)s * 36864u), UNI((s & 1) * 36864 + (wave + 8 * j) * 1024));
  };
  auto announce = [&](int done) {
    if (tid == 0) {
      st4c(rW, WS_PROG + (R_T5 + b) * 128, 0, (unsigned)done);
      if (exi) st4c(rW, WS_INI + (36 + b) * 128, 0, (unsigned)min(done + 16, NSEG - 1));
      STAMP_AT(R_T5 + b, done - 1);
    }
  };
  unsigned known = 0;
  bool ahead = false, owed = false;
  lptr kslot = lds + 90112;
  for (int s = 0; s < NSEG; ++s) {
    if (!ahead) {
      if (known < (unsigned)s + 1u) known = wait_known(rW, kslot + 8 + (s & 1) * 4, WS_LNP + (lane & 31) * 128, true, (unsigned)s + 1u);
      fetch(s);
    }
    if (s == 30 && tid == 0) STAMP_AT(R_T5 + b, 200);
    WAIT_VM0();
    WG_BAR();
    if (owed) announce(s);
    ahead = s + 1 < NSEG && known >= (unsigned)s + 2u;
    if (ahead) fetch(s + 1);
    unsigned pk = 0;
    if (!ahead) pk = peek_issue(rW, WS_LNP + (lane & 31) * 128);
    if (s == 30 && tid == 0) STAMP_AT(R_T5 + b, 201);
    f32x4 acc[2] = {f32x4{0, 0, 0, 0}, f32x4{0, 0, 0, 0}};
    proj_core(lds + (s & 1) * 36864, W, acc, lane);
    if (s == 30 && tid == 0) STAMP_AT(R_T5 + b, 202);
    lptr zb = lds + 73728;
#pragma unroll
    for (int tt = 0; tt < 2; ++tt) {
      const unsigned row = 16 * tt + i16, n = wave * 16 + 4 * g;
      float z[4];
#pragma unroll
      for (int r = 0; r < 4; ++r) z[r] = gelu_tanh(rbf(acc[tt][r]));
      lds_st(zb + row * 256 + (((n >> 3) ^ (row & 15)) << 4) + (n & 4) * 2, u32x2{pk2(z[0], z[1]), pk2(z[2], z[3])});
    }
    if (!ahead) peek_finish(kslot + (s & 1) * 4, pk);
    WG_BAR();
    if (!ahead && s + 1 < NSEG) {
      known = max(known, UNI(lds_ld<unsigned>(kslot + (s & 1) * 4)));
      if (known >= (unsigned)s + 2u) { ahead = true; fetch(s + 1); }
    }
    {
      const unsigned tok = tid >> 4, ch = tid & 15;
      const u32x4 w = lds_ld<u32x4>(zb + tok * 256 + ((ch ^ (tok & 15)) << 4));
      st16c(rW, tok * 9216u + ch * 16, UNI(WS_Z + (unsigned)s * 32u * 9216u + b * 256u), w);
      if (b >= 18) {
        float v[8];
        unpack8(w, v);
        float sum = ((v[0] + v[1]) + (v[2] + v[3])) + ((v[4] + v[5]) + (v[6] + v[7]));
        sum += dppf<0xB1>(sum); sum += dppf<0x4E>(sum); sum += dppf<0x141>(sum); sum += dppf<0x140>(sum);
        const float mean = sum * (1.0f / 128.0f);
        float m2 = 0.f;
#pragma unroll
        for (int j = 0; j < 8; ++j) { const float d = v[j] - mean; m2 += d * d; }
        m2 += dppf<0xB1>(m2); m2 += dppf<0x4E>(m2); m2 += dppf<0x141>(m2); m2 += dppf<0x140>(m2);
        st8c(rW, ch == 0 ? tok * 8u : 0x80000000u, UNI(WS_ZS + (unsigned)(s * 18 + b - 18) * 256u), u32x2{__builtin_bit_cast(unsigned, mean), __builtin_bit_cast(unsigned, m2)});
      }
    }
    if (exi) ex_mark(rW, exoff, pw, 36 + b, s + 16);
    owed = ahead;
    if (!ahead) {
      peek_finish(kslot + 16 + (s & 1) * 4, peek_issue(rW, WS_LNP + (lane & 31) * 128));
      WAIT_VM0(); WG_BAR(); announce(s + 1);
      known = max(known, UNI(lds_ld<unsigned>(kslot + 16 + (s & 1) * 4)));
    }
  }
}

__device__ __forceinline__ void qkv_role(const Params& p, rsrc_t rW, lptr lds, int nj) {
  const int tid = threadIdx.x, lane = tid & 63, wave = __builtin_amdgcn_readfirstlane(tid >> 6);
  u32x4 W[18];
  {
    const u16* src = (nj < 32 ? p.wq : (nj < 64 ? p.wk : p.wv)) + (size_t)(lane >> 4) * 4096 + (nj & 31) * 128 + (lane & 15) * 8;
#pragma unroll
    for (int j = 0; j < 18; ++j) glds16(src + (size_t)(wave * 18 + j) * 4 * 4096, (wave * 18 + j) * 1024);
    WAIT_VM0();
    WG_BAR();
    const int g = lane >> 4, q = (lane & 15) >> 2, pp = lane & 3;
#pragma unroll
    for (int ks = 0; ks < 18; ++ks) {
      const int row = 32 * ks + 8 * g + q, col = wave * 16 + 4 * pp;
      const u32x2 a0 = lds_tr(lds + row * 256 + col * 2), a1 = lds_tr(lds + (row + 4) * 256 + col * 2);
      W[ks] = u32x4{a0.x, a0.y, a1.x, a1.y};
    }
    WG_BAR();
  }
  unsigned so[5];
#pragma unroll
  for (int j = 0; j < 5; ++j) {
    const unsigned L = (wave + 8 * j) * 1024 + lane * 16, row = L / 1152u, ch = (L % 1152u) >> 4;
    so[j] = row * 1152u + ((ch ^ ((row >> 1) & 7)) << 4);
  }
  const u32x4 rs = mk_rs4(p.ws, (unsigned)(p.wsbytes > 0xFFFFFFF0ull ? 0xFFFFFFF0ull : p.wsbytes));
  auto fetch = [&](int s) {
#pragma unroll
    for (int j = 0; j < 5; ++j) if (wave + 8 * j < 36) blds16c(rs, so[j], UNI(WS_H + (unsigned)s * 36864u), UNI((s & 1) * 36864 + (wave + 8 * j) * 1024));
  };
  int rounds = (p.rolemask & 64u) ? 0 : 4;
  auto wait_h = [&](int s) {
    if (s == 0 && rounds == 0) {
      if (wave == 0) wait_ge(rW, WS_HP + (lane & 3) * 128, true, 1u);
      WG_BAR();
    } else if ((4 * s + 3) / 80 >= rounds) {
      rounds = (4 * s + 3) / 80 + 1;
      if (wave == 0) {
        wait_ge(rW, WS_HP + lane * 128, rounds < 4 || lane < 16, (unsigned)rounds);
        wait_ge(rW, WS_HP + (64 + (lane & 15)) * 128, rounds < 4, (unsigned)rounds);
      }
      WG_BAR();
    }
  };
  const int i16 = lane & 15, g = lane >> 4;
  wait_h(0);
  fetch(0);
  for (int s = 0; s < NSEG; ++s) {
    if (s < 3) WAIT_VM0(); else asm volatile("s_waitcnt vmcnt(1)" ::: "memory");
    WG_BAR();
    if (s > 0 && tid == 0) { st4c(rW, WS_PROG + (R_GQ + nj) * 128, 0, (unsigned)(s < 3 ? s : s - 1)); STAMP_AT(R_GQ + nj, s - 1); }
    if (s + 1 < NSEG) { wait_h(s + 1); fetch(s + 1); }
    lptr hb = lds + (s & 1) * 36864;
    f32x4 acc[2] = {f32x4{0, 0, 0, 0}, f32x4{0, 0, 0, 0}};
    proj_core(hb, W, acc, lane);
    lptr zb = lds + 73728 + (s & 1) * 8192;
#pragma unroll
    for (int tt = 0; tt < 2; ++tt) {
      const unsigned row = 16 * tt + i16, n = wave * 16 + 4 * g;
      lds_st(zb + row * 256 + (((n >> 3) ^ (row & 15)) << 4) + (n & 4) * 2, u32x2{pk2(acc[tt][0], acc[tt][1]), pk2(acc[tt][2], acc[tt][3])});
    }
    WG_BAR();
    {
      const unsigned tok = tid >> 4, ch = tid & 15;
      st16c(rW, tok * 24576u + ch * 16, UNI(WS_QKV + (unsigned)s * 32u * 24576u + nj * 256u), lds_ld<u32x4>(zb + tok * 256 + ((ch ^ (tok & 15)) << 4)));
    }
  }
  WAIT_VM0();
  WG_BAR();
  if (tid == 0) st4c(rW, WS_PROG + (R_GQ + nj) * 128, 0, (unsigned)NSEG);
}

constexpr unsigned G_WS = 0, G_Z = 32768, G_ZB = 14336, G_ST = 65536;
__device__ __forceinline__ void g_role(const Params& p, rsrc_t rW, lptr lds, int idx_) {
  const int tid = threadIdx.x, lane = tid & 63, wave = __builtin_amdgcn_readfirstlane(tid >> 6);
  const int mt = idx_ / 12, nt = idx_ % 12, t0 = 256 * mt, f0 = 192 * nt, wf = wave & 1, wt = wave >> 1;
  f32x16 acc[3][2];
#pragma unroll
  for (int i = 0; i < 3; ++i)
#pragma unroll
    for (int j = 0; j < 2; ++j)
#pragma unroll
      for (int r = 0; r < 16; ++r) acc[i][j][r] = 0.f;
  const unsigned zrow = tid / 12, zch = (tid % 12) * 2;
  const bool zact = tid < 384;
  const unsigned zvo = zact ? zrow * 9216u + (FH + f0) * 2u + zch * 16u : 0x80000000u;
  const GAS u16* wsrc = (const GAS u16*)p.wsp + (size_t)(t0 + (tid >> 2)) * S_ + (tid & 3) * 8;
  const unsigned wdst = (tid >> 2) * 64 + (((tid & 3) ^ ((tid >> 4) & 3)) << 4);
  u32x4 wr[2][2];
  unsigned known = 0;
  u32x4 z0, z1;
  auto load_w = [&](int s, auto R) { constexpr int r = decltype(R)::value; wr[r][0] = *(const GAS u32x4*)(wsrc + 32 * s); wr[r][1] = *(const GAS u32x4*)(wsrc + 32 * s + (size_t)128 * S_); };
  auto store_w = [&](int s, auto R) { constexpr int r = decltype(R)::value; lptr d = lds + G_WS + (s & 1) * 16384 + wdst; lds_st(d, wr[r][0]); lds_st(d + 128 * 64, wr[r][1]); };
  auto issue = [&](int s) {
    z0 = ld16c(rW, zvo, UNI(WS_Z + (unsigned)s * 32u * 9216u)); z1 = ld16c(rW, zact ? zvo + 16 : 0x80000000u, UNI(WS_Z + (unsigned)s * 32u * 9216u));
    if (wave == 7) {
      const unsigned tok = lane & 31, hf = lane >> 5;
      float m[9], q[9];
#pragma unroll
      for (int j = 0; j < 9; ++j) { const u32x2 v = ld8c(rW, tok * 8 + (hf * 9 + j) * 256, UNI(WS_ZS + (unsigned)s * 18u * 256u)); const unsigned vx = v.x, vy = v.y; m[j] = __builtin_bit_cast(float, vx); q[j] = __builtin_bit_cast(float, vy); }
      float sm = 0.f, sq = 0.f;
#pragma unroll
      for (int j = 0; j < 9; ++j) { sm += m[j]; sq += q[j]; }
      const float mean = xr32(sm, false) * (1.0f / 18.0f);
      float dv = 0.f;
#pragma unroll
      for (int j = 0; j < 9; ++j) { const float d = m[j] - mean; dv += d * d; }
      const float var = (xr32(sq, false) + 128.0f * xr32(dv, false)) * (1.0f / 2304.0f);
      const float sd = rbf(__builtin_sqrtf(rbf(rbf(var) + 1e-5f)));
      if (lane < 32) lds_st(lds + G_ST + (s & 1) * 256 + tok * 8, f32x2{rbf(mean), 1.0f / sd});
    }
  };
  auto finish = [&](int s) {
    if (zact) {
      const f32x2 st = lds_ld<f32x2>(lds + G_ST + (s & 1) * 256 + zrow * 8);
      float v[16];
      unpack8(z0, v); unpack8(z1, v + 8);
#pragma unroll
      for (int j = 0; j < 16; ++j) v[j] = rbf(v[j] - st[0]) * st[1];
      lptr d = lds + G_Z + (s & 1) * G_ZB + zrow * 448 + zch * 16;
      lds_st(d, u32x4{pk2(v[0], v[1]), pk2(v[2], v[3]), pk2(v[4], v[5]), pk2(v[6], v[7])});
      lds_st(d + 16, u32x4{pk2(v[8], v[9]), pk2(v[10], v[11]), pk2(v[12], v[13]), pk2(v[14], v[15])});
    }
  };
  auto wait_seg = [&](int s) {
    known = wait_known(rW, lds + G_ST + 512 + (s & 1) * 4, WS_PROG + (R_T5 + 18 + (lane < 18 ? lane : 0)) * 128, lane < 18, (unsigned)s + 1u);
  };
  const unsigned hh = lane >> 5, G = lane >> 4, q4 = (lane & 15) >> 2, p4 = lane & 3;
  auto compute = [&](int s) {
    lptr wb = lds + G_WS + (s & 1) * 16384, zb = lds + G_Z + (s & 1) * G_ZB;
    u32x4 bt[2][2]; u32x2 af[2][3][2];
#pragma unroll
    for (int ks = 0; ks < 2; ++ks) {
#pragma unroll
      for (int j = 0; j < 2; ++j) { const unsigned row = wt * 64 + j * 32 + (lane & 31); bt[ks][j] = lds_ld<u32x4>(wb + row * 64 + (((2 * ks + hh) ^ ((row >> 2) & 3)) << 4)); }
#pragma unroll
      for (int i = 0; i < 3; ++i) {
        lptr a = zb + (16 * ks + 8 * (G >> 1) + q4) * 448 + (wf * 96 + i * 32 + 16 * (G & 1) + 4 * p4) * 2;
        af[ks][i][0] = lds_tr(a); af[ks][i][1] = lds_tr(a + 4 * 448);
      }
    }
    SCHED();
#pragma unroll
    for (int ks = 0; ks < 2; ++ks)
#pragma unroll
      for (int i = 0; i < 3; ++i) {
        const u32x4 a4 = {af[ks][i][0].x, af[ks][i][0].y, af[ks][i][1].x, af[ks][i][1].y};
        acc[i][0] = mfma32(a4, bt[ks][0], acc[i][0]);
        acc[i][1] = mfma32(a4, bt[ks][1], acc[i][1]);
      }
  };
  WG_BAR();
  load_w(0, IC<0>{}); load_w(1, IC<1>{});
  wait_seg(0);
  issue(0);
  store_w(0, IC<0>{}); load_w(2, IC<0>{});
  WG_BAR();
  finish(0);
  WG_BAR();
  u32x4 zz[12];
  auto step = [&](int s, auto P) {
    constexpr int par = decltype(P)::value;
    const bool more = s + 1 < NSEG;
    const bool early = more && (unsigned)(s + 1) < known;
    if (early) issue(s + 1);
    compute(s);
    if (tid == 0) STAMP_AT(R_GQ + idx_, 100 + s);
    if (more) {
      if (!early) { wait_seg(s + 1); issue(s + 1); }
      WG_BAR();
      store_w(s + 1, IC<par ^ 1>{});
      if (s + 3 < NSEG) load_w(s + 3, IC<par ^ 1>{});
      finish(s + 1);
      if (s == NSEG - 2 && wave == 0) wait_ge(rW, WS_PROG + (R_T5 + (lane < 18 ? lane : 0)) * 128, lane < 18, (unsigned)NSEG);
      WG_BAR();
      if (s == NSEG - 2) {
#pragma unroll
        for (int j = 0; j < 12; ++j) { const unsigned id = tid + 512 * j, row = id / 24, ch = id % 24; zz[j] = ld16c(rW, row * 9216u + f0 * 2u + ch * 16u, UNI(WS_Z + (unsigned)t0 * 9216u)); }
      }
    }
  };
  for (int s = 0; s < NSEG - 2; s += 2) { step(s, IC<0>{}); step(s + 1, IC<1>{}); }
  step(NSEG - 2, IC<0>{}); step(NSEG - 1, IC<1>{});
  if (tid == 0) STAMP_AT(R_GQ + idx_, 170);
  WG_BAR();
#pragma unroll
  for (int j = 0; j < 2; ++j) {
    const unsigned trow = wt * 64 + j * 32 + (lane & 31);
    const float bb = bf2f(p.bsp[t0 + trow]);
#pragma unroll
    for (int i = 0; i < 3; ++i)
#pragma unroll
      for (int g = 0; g < 4; ++g) {
        float v[4];
#pragma unroll
        for (int r = 0; r < 4; ++r) v[r] = rbf(acc[i][j][4 * g + r]) + bb;
        lds_st(lds + trow * 400 + (wf * 96 + i * 32 + 8 * g + 4 * hh) * 2, u32x2{pk2(v[0], v[1]), pk2(v[2], v[3])});
      }
  }
  WG_BAR();
  {
#pragma unroll
    for (int j = 0; j < 12; ++j) {
      const unsigned id = tid + 512 * j, row = id / 24, ch = id % 24;
      const u32x4 gg = lds_ld<u32x4>(lds + row * 400 + ch * 16);
      const u32x4 o = {pk2(blo(zz[j].x) * blo(gg.x), bhi(zz[j].x) * bhi(gg.x)), pk2(blo(zz[j].y) * blo(gg.y), bhi(zz[j].y) * bhi(gg.y)),
                       pk2(blo(zz[j].z) * blo(gg.z), bhi(zz[j].z) * bhi(gg.z)), pk2(blo(zz[j].w) * blo(gg.w), bhi(zz[j].w) * bhi(gg.w))};
      st16c(rW, row * (FH * 2u) + f0 * 2u + ch * 16u, UNI(WS_Y + (unsigned)t0 * (FH * 2u)), o);
    }
  }
  WAIT_VM0();
  WG_BAR();
  if (tid == 0) st4c(rW, WS_YP + idx_ * 128, 0, 1u);
}

__device__ __forceinline__ void out_tile(const Params& p, rsrc_t rW, lptr lds, int xg, int r, bool early, int role) {
  const int tid = threadIdx.x, lane = tid & 63, wave = __builtin_amdgcn_readfirstlane(tid >> 6);
  const int tt = xg * 4 + r / 6, nt = r % 6, t0 = tt * 64, n0 = nt * 96;
  const int wt = wave & 1, wn = (wave >> 1) & 1, kh = wave >> 2;
  constexpr unsigned STG = 40960;
  unsigned yo[2], wo[3];
#pragma unroll
  for (int j = 0; j < 2; ++j) { const unsigned row = (wave * 2 + j) * 4 + (lane >> 4); yo[j] = row * (FH * 2u) + (((lane & 15) ^ (row & 15)) << 4); }
#pragma unroll
  for (int j = 0; j < 3; ++j) { const unsigned L = (wave * 3 + j) * 1024 + lane * 16, row = L / 192u, ch = (L % 192u) >> 4; wo[j] = row * 1152u + ((ch ^ (((row >> 3) & 1) << 1)) << 4); }
  const u32x4 ry = mk_rs4(p.ws + WS_Y, S_ * FH * 2), rw = mk_rs4(p.wout, FH * 576 * 2);
  const unsigned ys0 = UNI(t0 * (FH * 2u)), ws0 = UNI(n0 * 2u);
  auto stage_w = [&](int kt, int buf) {
#pragma unroll
    for (int j = 0; j < 3; ++j) blds16p(rw, wo[j], ws0 + kt * 128 * 1152, UNI(buf * STG + 16384 + (wave * 3 + j) * 1024));
  };
  auto stage_y = [&](int kt, int buf) {
#pragma unroll
    for (int j = 0; j < 2; ++j) blds16c(ry, yo[j], ys0 + kt * 256, UNI(buf * STG + (wave * 2 + j) * 1024));
  };
  f32x4 acc[2][3];
#pragma unroll
  for (int i = 0; i < 2; ++i)
#pragma unroll
    for (int j = 0; j < 3; ++j) acc[i][j] = f32x4{0, 0, 0, 0};
  const int i16 = lane & 15, g = lane >> 4, q = i16 >> 2, pp = lane & 3;
  WG_BAR();
  if (early) {
    for (int kt = 17; kt >= 4; --kt) stage_w(kt, kt & 3);
    WAIT_VM0();
    WG_BAR();
  }
  stage_w(0, 0); stage_w(1, 1); stage_w(2, 2); stage_w(3, 3);
  if (wave == 0) wait_ge(rW, WS_YP + ((tt >> 2) * 12 + (lane < 12 ? lane : 0)) * 128, lane < 12, 1u);
  WG_BAR();
  if (tid == 0) STAMP_AT(role, 252);
  stage_y(0, 0); stage_y(1, 1); stage_y(2, 2); stage_y(3, 3);
  u32x4 bt[2][2][2]; u32x2 af[2][2][3][2];
  auto reads = [&](int kt, auto R) {
    constexpr int rr = decltype(R)::value;
    lptr yb = lds + (kt & 3) * STG, wb = yb + 16384;
#pragma unroll
    for (int k2 = 0; k2 < 2; ++k2) {
      const int ks = kh * 2 + k2;
#pragma unroll
      for (int i = 0; i < 2; ++i) { const unsigned row = wt * 32 + i * 16 + i16; bt[rr][k2][i] = lds_ld<u32x4>(yb + row * 256 + (((4 * ks + g) ^ (row & 15)) << 4)); }
#pragma unroll
      for (int j = 0; j < 3; ++j) {
        lptr a = wb + (32 * ks + 8 * g + q) * 192 + ((((3 * wn + j) ^ (g & 1)) * 16 + 4 * pp) * 2);
        af[rr][k2][j][0] = lds_tr(a); af[rr][k2][j][1] = lds_tr(a + 4 * 192);
      }
    }
  };
  auto step = [&](int kt, auto R) {
    constexpr int rr = decltype(R)::value;
    if (kt == 0) asm volatile("s_waitcnt vmcnt(4)" ::: "memory");
    else if (kt == 1) asm volatile("s_waitcnt vmcnt(7)" ::: "memory");
    else if (kt < 14) asm volatile("s_waitcnt vmcnt(10)" ::: "memory");
    else if (kt == 14) asm volatile("s_waitcnt vmcnt(10)" ::: "memory");
    else if (kt == 15) asm volatile("s_waitcnt vmcnt(5)" ::: "memory");
    else if (kt == 16) asm volatile("s_waitcnt vmcnt(0)" ::: "memory");
    WG_BAR();
    if (kt + 1 < 18) reads(kt + 1, IC<rr ^ 1>{});
    SCHED();
    if (kh == 0 && kt + 4 < 18) { stage_w(kt + 4, kt & 3); stage_y(kt + 4, kt & 3); }
    SCHED();
#pragma unroll
    for (int k2 = 0; k2 < 2; ++k2)
#pragma unroll
      for (int j = 0; j < 3; ++j) {
        const u32x4 a4 = {af[rr][k2][j][0].x, af[rr][k2][j][0].y, af[rr][k2][j][1].x, af[rr][k2][j][1].y};
        acc[0][j] = mfma16(a4, bt[rr][k2][0], acc[0][j]);
        acc[1][j] = mfma16(a4, bt[rr][k2][1], acc[1][j]);
      }
    SCHED();
    if (kh == 1 && kt + 4 < 18) { stage_w(kt + 4, kt & 3); stage_y(kt + 4, kt & 3); }
  };
  asm volatile("s_waitcnt vmcnt(6)" ::: "memory");
  WG_BAR();
  reads(0, IC<0>{});
  for (int kt = 0; kt < 16; kt += 2) { step(kt, IC<0>{}); step(kt + 1, IC<1>{}); }
  step(16, IC<0>{});
  u32x2 xres[2][3]; f32x4 gbv[3];
#pragma unroll
  for (int j = 0; j < 3; ++j) {
    const int n = n0 + wn * 48 + j * 16 + 4 * g;
    gbv[j] = __builtin_bit_cast(f32x4, ld16c(rW, (unsigned)n * 4u, WS_MOD + 5 * 576 * 4));
#pragma unroll
    for (int i = 0; i < 2; ++i) xres[i][j] = ld8c(rW, (unsigned)((t0 + wt * 32 + i * 16 + i16) * 576 + n) * 2u, WS_X1);
  }
  step(17, IC<1>{});
  WG_BAR();
  if (tid == 0) STAMP_AT(role, 253);
  lptr red = lds + ((wave & 3) * 6 * 64 + lane) * 16;
  if (kh == 1) {
#pragma unroll
    for (int i = 0; i < 2; ++i)
#pragma unroll
      for (int j = 0; j < 3; ++j) lds_st(red + (i * 3 + j) * 1024, acc[i][j]);
  }
  WG_BAR();
  if (kh == 0) {
#pragma unroll
    for (int i = 0; i < 2; ++i)
#pragma unroll
      for (int j = 0; j < 3; ++j) {
        const f32x4 a = acc[i][j] + lds_ld<f32x4>(red + (i * 3 + j) * 1024);
        const int t = t0 + wt * 32 + i * 16 + i16, n = n0 + wn * 48 + j * 16 + 4 * g;
        const u32x2 xv = xres[i][j];
        const f32x4 gb = gbv[j];
        const float o0 = blo(xv.x) + rbf(gb[0] * rbf(a[0])), o1 = bhi(xv.x) + rbf(gb[1] * rbf(a[1]));
        const float o2 = blo(xv.y) + rbf(gb[2] * rbf(a[2])), o3 = bhi(xv.y) + rbf(gb[3] * rbf(a[3]));
        *(GAS u32x2*)(p.out + (size_t)t * 576 + n) = u32x2{pk2(o0, o1), pk2(o2, o3)};
      }
  }
}

constexpr int CODE_BYTES = 131072;
__global__ void __launch_bounds__(512, 2) k_mega(Params p) {
  extern __shared__ __attribute__((aligned(16))) unsigned char smem[];
  lptr lds = (lptr)smem;
  const int tid = threadIdx.x;
  const rsrc_t rW = mk_rsrc(p.ws, p.wsbytes);
  if (!(p.tune2 & 512u) && tid >= 448) {
    unsigned long long pc;
    asm volatile("s_getpc_b64 %0" : "=s"(pc));
    const GAS u32x4* code = (const GAS u32x4*)((pc & ~63ull) + (unsigned long long)(blockIdx.x >> 3) * (CODE_BYTES / 32)) + (tid & 63);
    unsigned x = 0;
#pragma unroll
    for (int j = 0; j < CODE_BYTES / 32 / 1024; ++j) x ^= code[j * 64].x;
    asm volatile("" :: "v"(x));
  }
  if (tid == 0) {
    const unsigned x = (unsigned)__builtin_amdgcn_s_getreg(20 | (3 << 11)) & 15u;
    GAS unsigned* tick = (GAS unsigned*)(p.ws + WS_TICK);
    unsigned r = __hip_atomic_fetch_add(tick + x * 32, 1u, __ATOMIC_RELAXED, __HIP_MEMORY_SCOPE_AGENT);
    unsigned slot;
    if (x < 8u && r < 32u) slot = x * 32u + r;
    else {
      unsigned k = __hip_atomic_fetch_add((GAS unsigned*)(p.ws + WS_OVF), 1u, __ATOMIC_RELAXED, __HIP_MEMORY_SCOPE_AGENT);
      slot = 0;
      for (unsigned sp = 0; sp < SPIN_CAP * 8u; ++sp) {
        unsigned sum = 0;
        for (int j = 0; j < 16; ++j) sum += __hip_atomic_load(tick + j * 32, __ATOMIC_RELAXED, __HIP_MEMORY_SCOPE_AGENT);
        if (sum >= gridDim.x) break;
        __builtin_amdgcn_s_sleep(8);
      }
      for (unsigned j = 0; j < 8u; ++j) {
        const unsigned n = min(__hip_atomic_load(tick + j * 32, __ATOMIC_RELAXED, __HIP_MEMORY_SCOPE_AGENT), 32u), fr = 32u - n;
        if (k < fr) { slot = j * 32u + n + k; break; }
        k -= fr;
      }
    }
    const unsigned xs = slot >> 5, rs = slot & 31u;
    unsigned role;
    if (xs < 4u) role = rs < 16u ? xs * 16u + rs : R_GQ + xs * 16u + (rs - 16u);
    else { const unsigned k = (rs << 2) | (xs - 4u); role = k < 80u ? R_T4 + k : R_GQ + 64u + (k - 80u); }
    lds_st(lds, role | (slot << 8));
  }
  __syncthreads();
  const int rs_ = __builtin_amdgcn_readfirstlane((int)lds_ld<unsigned>(lds));
  const int role = rs_ & 255, slot = rs_ >> 8;
  __syncthreads();
  const unsigned long long wsa = (unsigned long long)p.ws;
  const unsigned exoff = (unsigned)(((wsa + WS_EX + 2097151ull) & ~2097151ull) - wsa);

  if (role < R_T4) {
    if (p.rolemask & 1u) {
      if (tid < 256) chain_path<true>(p, rW, lds, role, exoff); else chain_path<false>(p, rW, lds, role, exoff);
    }
  } else if (role < R_T5) {
    if (p.rolemask & 4u) t4_role(p, rW, lds, role - R_T4, exoff); else { if (p.rolemask & 64u) prologue(p, rW, lds, role - R_T4); if (p.rolemask & 1u) ex_init(rW, exoff, ex_parity(rW, tid & 63), role - R_T4, 0, NSEG); }
  } else if (role < R_LN) {
    if (p.rolemask & 16u) t5_role(p, rW, lds, role - R_T5, exoff); else { if (p.rolemask & 64u) prologue(p, rW, lds, role - R_T5 + 36); if (p.rolemask & 1u) ex_init(rW, exoff, ex_parity(rW, tid & 63), role - R_T5 + 36, 0, NSEG); }
  } else if (role < R_GQ) {
    if (p.rolemask & 8u) ln_role(p, rW, lds, role - R_LN, exoff); else { if (p.rolemask & 64u) prologue(p, rW, lds, role - R_LN + 72); if (p.rolemask & 1u) ex_init(rW, exoff, ex_parity(rW, tid & 63), role - R_LN + 72, 0, NSEG); }
  } else {
    const int gi = role - R_GQ;
    if (gi >= 96 && gi < 100 && (p.rolemask & 1u)) ex_measure(rW, exoff, gi - 96);
    if ((p.rolemask & 2u) && gi < 96) {
      const int x = gi < 64 ? gi >> 4 : (gi - 64) >> 3, li = gi < 64 ? gi & 15 : 16 + ((gi - 64) & 7);
      qkv_role(p, rW, lds, (li % 3) * 32 + x * 8 + li / 3);
    }
    if (gi >= 96 && (p.rolemask & 2u) && !(p.tune2 & 64u)) {
      for (int i = 0; i < 15; ++i) __builtin_amdgcn_s_sleep(127);
      const int k = gi - 96;
      const u32x4* a = (const u32x4*)p.wsp; const u32x4* b = (const u32x4*)p.wout;
      unsigned acc = 0;
      for (int i = k * 512 + tid; i < S_ * S_ / 8; i += 16 * 512 * 4) {
        const u32x4 v0 = *(const GAS u32x4*)(a + i), v1 = *(const GAS u32x4*)(a + min(i + 8192, S_ * S_ / 8 - 1)), v2 = *(const GAS u32x4*)(a + min(i + 16384, S_ * S_ / 8 - 1)), v3 = *(const GAS u32x4*)(a + min(i + 24576, S_ * S_ / 8 - 1));
        acc ^= v0.x ^ v1.y ^ v2.z ^ v3.w;
      }
      for (int i = k * 512 + tid; i < FH * D_ / 8; i += 16 * 512) acc ^= (*(const GAS u32x4*)(b + i)).x;
      asm volatile("" :: "v"(acc));
    }
    if ((p.rolemask & 32u) && gi < N_G) g_role(p, rW, lds, gi);
  }
  STAMP_AT(role, 250);
  if ((p.rolemask & 128u) && (slot & 31) < 24) out_tile(p, rW, lds, slot >> 5, slot & 31, (role < R_GQ || role >= R_GQ + N_G) && !(p.tune2 & 128u), role);
  STAMP_AT(role, 251);
}

extern "C" void kernel_launch_dbg(void* const* d_in, void* d_out, void* d_ws, size_t ws_size, hipStream_t stream, unsigned stages, unsigned rolemask, unsigned t0, unsigned t1, unsigned t2) {
  Params p;
  p.x = (const u16*)d_in[0]; p.mem0 = (const u16*)d_in[1]; p.wq = (const u16*)d_in[2]; p.wk = (const u16*)d_in[3]; p.wv = (const u16*)d_in[4]; p.wo = (const u16*)d_in[5];
  p.win = (const u16*)d_in[8]; p.wsp = (const u16*)d_in[9]; p.bsp = (const u16*)d_in[10]; p.wout = (const u16*)d_in[11]; p.c = (const u16*)d_in[12]; p.wmod = (const u16*)d_in[13];
  p.ws = (unsigned char*)d_ws; p.out = (u16*)d_out; p.wsbytes = ws_size; p.rolemask = rolemask; p.tune0 = t0; p.tune1 = t1; p.tune2 = t2;
  if (stages & 1u) (void)hipMemsetAsync(d_ws, 0, WS_CTRL_BYTES, stream);
  if (stages & 16u) { (void)hipMemsetAsync((char*)d_ws + WS_TICK, 0, WS_LNP - WS_TICK, stream); (void)hipMemsetAsync((char*)d_ws + WS_INI, 0, 80 * 128, stream); }
  if (stages & 2u) {
    k_mod<<<108, 256, 0, stream>>>((const u16*)d_in[12], (const u16*)d_in[13], (float*)((char*)d_ws + WS_MOD));
    k_ln1<<<256, 512, 0, stream>>>(p.x, (const float*)((char*)d_ws + WS_MOD), (u16*)((char*)d_ws + WS_H));
  }
  if (stages & 4u) {
    (void)hipFuncSetAttribute((const void*)k_mega, hipFuncAttributeMaxDynamicSharedMemorySize, 163840);
    k_mega<<<256, 512, 163840, stream>>>(p);
  }
}
extern "C" void kernel_launch(void* const* d_in, const int* in_sizes, int n_in, void* d_out, int out_size, void* d_ws, size_t ws_size, hipStream_t stream) {
  kernel_launch_dbg(d_in, d_out, d_ws, ws_size, stream, 5u, 0xffu, 10u, 4u, 0u);
}
```
